```python
import torch

def _softmax(x, axis=-1):
    return torch.softmax(x, dim=axis)
def _gelu(x):
    return torch.nn.functional.gelu(x, approximate="tanh")
def _silu(x):
    return torch.nn.functional.silu(x)
def _sigmoid(x):
    return torch.sigmoid(x)
def _layernorm(x, axis=-1, eps=1e-5):
    m = x.mean(dim=axis, keepdim=True)
    v = x.var(dim=axis, keepdim=True, unbiased=False)
    return (x - m) / torch.sqrt(v + eps)
def _scatter_add(tgt, idx, src, axis=0):
    return tgt.clone().index_add_(axis, idx, src)
def _segment_sum(src, seg, n):
    out = torch.zeros((n,) + src.shape[1:], dtype=src.dtype)
    return out.index_add_(0, seg, src)
def _pad(x, pw):
    flat = tuple(v for pair in reversed(pw) for v in pair)
    return torch.nn.functional.pad(x, flat)

kernel_name = 'gated_mix__linear_kernel_hedgehog__linear_kernel_hedgehog__prompt'

def setup_inputs():

    _w = (lambda a: (lambda t: t.to(torch.bfloat16) if t.is_floating_point() else t)(torch.as_tensor(a)))
    def _A_setup():
        B, S, D, H = 1, 2048, 576, 32
        Dh = D // H
        return {
            "x": _w((lambda *s: torch.randn(*s, dtype=torch.bfloat16) * 0.05)(B, S, D)),
            "wqkv": _w((lambda *s: torch.randn(*s, dtype=torch.bfloat16) * 0.8)(D, 3 * D)),
            "w_phi": _w((lambda *s: torch.randn(*s, dtype=torch.bfloat16) * 0.8)(H, Dh, Dh) * (Dh ** -0.5)),
            "wo": _w((lambda *s: torch.randn(*s, dtype=torch.bfloat16) * 0.05)(D, D)),
        }
    _A_in = _A_setup()

    def _B_setup():
        B, S, D, H = 1, 2048, 576, 32
        Dh = D // H
        return {
            "x": _w((lambda *s: torch.randn(*s, dtype=torch.bfloat16) * 0.05)(B, S, D)),
            "wqkv": _w((lambda *s: torch.randn(*s, dtype=torch.bfloat16) * 0.8)(D, 3 * D)),
            "w_phi": _w((lambda *s: torch.randn(*s, dtype=torch.bfloat16) * 0.8)(H, Dh, Dh) * (Dh ** -0.5)),
            "wo": _w((lambda *s: torch.randn(*s, dtype=torch.bfloat16) * 0.05)(D, D)),
        }
    _B_in = _B_setup()

    return {
        "A_x": _A_in["x"],
        "A_wqkv": _A_in["wqkv"],
        "A_w_phi": _A_in["w_phi"],
        "A_wo": _A_in["wo"],
        "B_x": _B_in["x"],
        "B_wqkv": _B_in["wqkv"],
        "B_w_phi": _B_in["w_phi"],
        "B_wo": _B_in["wo"],
        "gate_w": _w((lambda *s: torch.randn(*s, dtype=torch.bfloat16) * 0.05)(576) * 16.0),
    }

def reference(A_x, A_wqkv, A_w_phi, A_wo, B_x, B_wqkv, B_w_phi, B_wo, gate_w):
    def _A_ref(x, wqkv, w_phi, wo):
        B, S, D = x.shape
        H = 32; Dh = D // H
        qkv = (lambda x, *d: x.permute(*d))((x @ wqkv).reshape(B, S, 3, H, Dh), 2, 0, 3, 1, 4)
        q, k, v = qkv[0], qkv[1], qkv[2]
        pq = torch.einsum("bhsd,hdf->bhsf", q, w_phi)
        pk = torch.einsum("bhsd,hdf->bhsf", k, w_phi)
        phi_q = torch.concatenate([_softmax(pq, axis=-1), _softmax(-pq, axis=-1)], axis=-1)
        phi_k = torch.concatenate([_softmax(pk, axis=-1), _softmax(-pk, axis=-1)], axis=-1)
        kv = torch.einsum("bhsf,bhsd->bhfd", phi_k, v)
        z  = torch.einsum("bhsf->bhf", phi_k)
        num = torch.einsum("bhqf,bhfd->bhqd", phi_q, kv)
        den = torch.einsum("bhqf,bhf->bhq", phi_q, z)[..., None] + 1e-6
        o = num / den
        return (lambda x, *d: x.permute(*d))(o, 0, 2, 1, 3).reshape(B, S, D) @ wo

    def _B_ref(x, wqkv, w_phi, wo):
        B, S, D = x.shape
        H = 32; Dh = D // H
        qkv = (lambda x, *d: x.permute(*d))((x @ wqkv).reshape(B, S, 3, H, Dh), 2, 0, 3, 1, 4)
        q, k, v = qkv[0], qkv[1], qkv[2]
        pq = torch.einsum("bhsd,hdf->bhsf", q, w_phi)
        pk = torch.einsum("bhsd,hdf->bhsf", k, w_phi)
        phi_q = torch.concatenate([_softmax(pq, axis=-1), _softmax(-pq, axis=-1)], axis=-1)
        phi_k = torch.concatenate([_softmax(pk, axis=-1), _softmax(-pk, axis=-1)], axis=-1)
        kv = torch.einsum("bhsf,bhsd->bhfd", phi_k, v)
        z  = torch.einsum("bhsf->bhf", phi_k)
        num = torch.einsum("bhqf,bhfd->bhqd", phi_q, kv)
        den = torch.einsum("bhqf,bhf->bhq", phi_q, z)[..., None] + 1e-6
        o = num / den
        return (lambda x, *d: x.permute(*d))(o, 0, 2, 1, 3).reshape(B, S, D) @ wo

    x = A_x
    g = _sigmoid((x * gate_w).sum(-1, keepdims=True))
    a = _A_ref(x, A_wqkv, A_w_phi, A_wo)
    b = _B_ref(x, B_wqkv, B_w_phi, B_wo)
    return g * a + (1 - g) * b
```

```cpp
#include <hip/hip_runtime.h>
#include <hip/hip_bf16.h>
typedef __hip_bfloat16 bf16;
constexpr int S=2048, D=576, H=32, DH=18, N3=1728;
__device__ inline float b2f(bf16 v){return __bfloat162float(v);}
__device__ inline float rb(float v){return __bfloat162float(__float2bfloat16(v));}

__global__ void k_qkv(const bf16* x, const bf16* wA, const bf16* wB, float* qkv){
  int n = blockIdx.x*blockDim.x+threadIdx.x; int s = blockIdx.y; int br = blockIdx.z;
  if(n>=N3) return;
  const bf16* w = br? wB: wA;
  float acc=0.f;
  for(int k=0;k<D;k++) acc += b2f(x[s*D+k])*b2f(w[k*N3+n]);
  qkv[((size_t)br*S+s)*N3+n]=rb(acc);
}
__global__ void k_phi(const float* qkv, const bf16* pA, const bf16* pB, float* phi){
  int idx = blockIdx.x*blockDim.x+threadIdx.x;
  if(idx>=2*S*2*H) return;
  int h = idx%H, part=(idx/H)%2, s=(idx/(2*H))%S, br=idx/(2*H*S);
  const bf16* wp = (br? pB:pA) + h*DH*DH;
  const float* q = qkv + ((size_t)br*S+s)*N3 + part*D + h*DH;
  float p[DH];
  float mx=-1e30f, mn=1e30f;
  for(int f=0;f<DH;f++){ float a=0; for(int d=0;d<DH;d++) a+=q[d]*b2f(wp[d*DH+f]); p[f]=a; mx=fmaxf(mx,a); mn=fminf(mn,a);}
  float s1=0,s2=0; float e1[DH],e2[DH];
  for(int f=0;f<DH;f++){ e1[f]=__expf(p[f]-mx); e2[f]=__expf(mn-p[f]); s1+=e1[f]; s2+=e2[f]; }
  float* o = phi + (size_t)idx*36;
  for(int f=0;f<DH;f++){ o[f]=e1[f]/s1; o[DH+f]=e2[f]/s2; }
}
__global__ void k_kv(const float* qkv, const float* phi, float* kv){
  int bh = blockIdx.x; int br=bh/H, h=bh%H;
  int t = threadIdx.x; if(t>=36*19) return;
  int f=t/19, d=t%19;
  float a=0;
  for(int s=0;s<S;s++){
    float pk = phi[((((size_t)br*S+s)*2+1)*H+h)*36+f];
    float v = d<18 ? qkv[((size_t)br*S+s)*N3+2*D+h*DH+d] : 1.f;
    a+=pk*v;
  }
  kv[(size_t)bh*36*19+t]=a;
}
__global__ void k_o(const bf16* x, const bf16* gw, const float* phi, const float* kv, float* o){
  int idx = blockIdx.x*blockDim.x+threadIdx.x;
  if(idx>=S*2*H) return;
  int h=idx%H, br=(idx/H)%2, s=idx/(2*H);
  float g=0; for(int k=0;k<D;k++) g+=b2f(x[s*D+k])*b2f(gw[k]);
  g = 1.f/(1.f+__expf(-g)); if(br) g=1.f-g;
  const float* pq = phi + ((((size_t)br*S+s)*2+0)*H+h)*36;
  const float* K = kv + (size_t)(br*H+h)*36*19;
  float den=1e-6f; for(int f=0;f<36;f++) den+=pq[f]*K[f*19+18];
  for(int d=0;d<DH;d++){ float a=0; for(int f=0;f<36;f++) a+=pq[f]*K[f*19+d]; o[(size_t)s*1152+br*D+h*DH+d]=g*a/den; }
}
__global__ void k_out(const float* o, const bf16* woA, const bf16* woB, bf16* out){
  int n = blockIdx.x*blockDim.x+threadIdx.x; int s=blockIdx.y; if(n>=D) return;
  float acc=0;
  for(int k=0;k<D;k++) acc+=o[(size_t)s*1152+k]*b2f(woA[k*D+n]);
  for(int k=0;k<D;k++) acc+=o[(size_t)s*1152+D+k]*b2f(woB[k*D+n]);
  out[s*D+n]=__float2bfloat16(acc);
}
extern "C" void kernel_launch(void* const* d_in, const int* in_sizes, int n_in,
                              void* d_out, int out_size, void* d_ws, size_t ws_size,
                              hipStream_t stream) {
  const bf16* x=(const bf16*)d_in[0]; const bf16* wA=(const bf16*)d_in[1]; const bf16* pA=(const bf16*)d_in[2]; const bf16* woA=(const bf16*)d_in[3];
  const bf16* wB=(const bf16*)d_in[5]; const bf16* pB=(const bf16*)d_in[6]; const bf16* woB=(const bf16*)d_in[7]; const bf16* gw=(const bf16*)d_in[8];
  float* qkv=(float*)d_ws; float* phi=qkv+(size_t)2*S*N3; float* kv=phi+(size_t)2*S*2*H*36; float* o=kv+2*H*36*19+64;
  k_qkv<<<dim3((N3+255)/256,S,2),256,0,stream>>>(x,wA,wB,qkv);
  k_phi<<<(2*S*2*H+255)/256,256,0,stream>>>(qkv,pA,pB,phi);
  k_kv<<<2*H,704,0,stream>>>(qkv,phi,kv);
  k_o<<<(S*2*H+255)/256,256,0,stream>>>(x,gw,phi,kv,o);
  k_out<<<dim3((D+255)/256,S),256,0,stream>>>(o,woA,woB,(bf16*)d_out);
}
```

```cpp
#include <hip/hip_runtime.h>
#include <stdint.h>

typedef short s16x4 __attribute__((ext_vector_type(4)));
typedef short s16x8 __attribute__((ext_vector_type(8)));
typedef float f32x4 __attribute__((ext_vector_type(4)));
typedef float f32x2 __attribute__((ext_vector_type(2)));
typedef unsigned u32x4 __attribute__((ext_vector_type(4)));
typedef unsigned u32x2 __attribute__((ext_vector_type(2)));
typedef __bf16 bf16x2_t __attribute__((ext_vector_type(2)));
typedef __amdgpu_buffer_rsrc_t rsrc_t;
#define LAS __attribute__((address_space(3)))
#define DI __device__ __forceinline__

constexpr unsigned SLOT = 35840, XB = 16384, WBLK = 2432;
constexpr unsigned L_PHI = 82944, L_V = L_PHI + 36 * 1024;
constexpr unsigned L_A0 = 143360, L_A1 = L_A0 + 6144, L_GATE = L_A1 + 3072;
constexpr unsigned L_WPHI = L_GATE + 1152, L_KVT = L_WPHI, L_EP = L_KVT + 7680;
constexpr unsigned LDS_BYTES = L_EP + 16;
constexpr unsigned WS_EP = 0, WS_FLAG = 4096, WS_H2 = 32768, WS_H1 = 1u << 20, WS_O = 8u << 20, WS_BYTES = 16u << 20;
constexpr unsigned OOB = 0x80000000u;
constexpr int NUNITS = 1368, SLICE = 88;
constexpr unsigned SPIN_CAP = 1u << 18;

DI void dma16(rsrc_t rs, unsigned voff, unsigned soff, unsigned ldsaddr) {
  asm volatile("s_mov_b32 m0, %0\n\ts_nop 3\n\tbuffer_load_dwordx4 %1, %2, %3 offen lds"
               :: "s"(ldsaddr), "v"(voff), "s"(rs), "s"(soff) : "memory");
}
template <int N> DI void wait_bar() { asm volatile("s_waitcnt vmcnt(%0) lgkmcnt(0)\n\ts_barrier" :: "i"(N) : "memory"); }
DI void bar() { asm volatile("s_waitcnt lgkmcnt(0)\n\ts_barrier" ::: "memory"); }
DI void drain() { asm volatile("s_waitcnt vmcnt(0)" ::: "memory"); }

DI unsigned pk2(float a, float b) { f32x2 v = {a, b}; return __builtin_bit_cast(unsigned, __builtin_convertvector(v, bf16x2_t)); }
DI s16x8 mk8(unsigned a, unsigned b, unsigned c, unsigned d) { u32x4 v = {a, b, c, d}; return __builtin_bit_cast(s16x8, v); }
DI s16x4 mk4(unsigned a, unsigned b) { u32x2 v = {a, b}; return __builtin_bit_cast(s16x4, v); }
DI f32x4 mfma32(s16x8 a, s16x8 b, f32x4 c) { return __builtin_amdgcn_mfma_f32_16x16x32_bf16(a, b, c, 0, 0, 0); }
DI s16x4 trd(LAS char* L, unsigned a) { return __builtin_amdgcn_ds_read_tr16_b64_v4i16((LAS s16x4*)(L + a)); }
DI s16x8 trd2(LAS char* L, unsigned a0, unsigned a1) {
  s16x4 lo = trd(L, a0), hi = trd(L, a1);
  return __builtin_shufflevector(lo, hi, 0, 1, 2, 3, 4, 5, 6, 7);
}
DI s16x8 rd128(LAS char* L, unsigned a) { return *(LAS s16x8*)(L + a); }
DI s16x4 rd64(LAS char* L, unsigned a) { return *(LAS s16x4*)(L + a); }
struct F2 { float a, b; };
DI F2 sw16(float x) { auto r = __builtin_amdgcn_permlane16_swap(__float_as_uint(x), __float_as_uint(x), false, false);
  return F2{__uint_as_float(r[0]), __uint_as_float(r[1])}; }
DI F2 sw32(float x) { auto r = __builtin_amdgcn_permlane32_swap(__float_as_uint(x), __float_as_uint(x), false, false);
  return F2{__uint_as_float(r[0]), __uint_as_float(r[1])}; }

DI void featmap(const f32x4 p0, const f32x4 p1, const f32x4 p2, unsigned (&ph)[9]) {
  const float v[9] = {p0[0], p0[1], p0[2], p0[3], p1[0], p1[1], p1[2], p1[3], p2[0]};
  float mx = fmaxf(fmaxf(fmaxf(v[0], v[1]), fmaxf(v[2], v[3])), fmaxf(fmaxf(v[4], v[5]), fmaxf(fmaxf(v[6], v[7]), v[8])));
  float mn = fminf(fminf(fminf(v[0], v[1]), fminf(v[2], v[3])), fminf(fminf(v[4], v[5]), fminf(fminf(v[6], v[7]), v[8])));
  { F2 r = sw16(mx); mx = fmaxf(r.a, r.b); }
  { F2 r = sw16(mn); mn = fminf(r.a, r.b); }
  const float c = 1.44269504088896340736f;
  const float mxc = -mx * c, mnc = mn * c;
  float e[18];
  float sp = 0.f, sn = 0.f;
#pragma unroll
  for (int i = 0; i < 9; ++i) {
    e[i] = __builtin_amdgcn_exp2f(fmaf(v[i], c, mxc));
    e[9 + i] = __builtin_amdgcn_exp2f(fmaf(v[i], -c, mnc));
    sp += e[i]; sn += e[9 + i];
  }
  { F2 r = sw16(sp); sp = r.a + r.b; }
  { F2 r = sw16(sn); sn = r.a + r.b; }
  const float rp = __builtin_amdgcn_rcpf(sp), rn = __builtin_amdgcn_rcpf(sn);
#pragma unroll
  for (int i = 0; i < 9; ++i) { e[i] *= rp; e[9 + i] *= rn; }
#pragma unroll
  for (int m = 0; m < 9; ++m) ph[m] = pk2(e[2 * m], e[2 * m + 1]);
}

DI void transform(LAS char* L, int hp, unsigned lane, const f32x4 t0, const f32x4 t1, const f32x4 t2, f32x4 (&p)[3]) {
  const s16x8 b0 = mk8(pk2(t0[0], t0[1]), pk2(t0[2], t0[3]), pk2(t1[0], t1[1]), pk2(t1[2], t1[3]));
  const s16x8 b1 = mk8(pk2(t2[0], t2[1]), pk2(t2[2], t2[3]), 0u, 0u);
#pragma unroll
  for (int u = 0; u < 3; ++u) {
    const s16x8 a0 = rd128(L, L_A0 + (hp * 3 + u) * 1024 + lane * 16);
    const u32x2 a1 = *(LAS u32x2*)(L + L_A1 + (hp * 3 + u) * 512 + lane * 8);
    f32x4 z = {0.f, 0.f, 0.f, 0.f};
    z = mfma32(a0, b0, z);
    p[u] = mfma32(mk8(a1[0], a1[1], 0u, 0u), b1, z);
  }
}

DI unsigned g24(float v, unsigned tlo) { return ((__float_as_uint(v) + 0x80u) >> 8) | tlo; }
DI float ung24(unsigned lo) { return __uint_as_float(lo << 8); }
DI bool gok(const u32x4 v, unsigned tlo, unsigned thi) {
  return v[1] == thi && v[3] == thi && (v[0] & 0xff000000u) == tlo && (v[2] & 0xff000000u) == tlo;
}

template <int NT>
DI void p3_body(LAS char* L, rsrc_t rws, unsigned lane, int kq, int tau0, unsigned elo, unsigned ehi, f32x4 (&acc)[3][4]) {
  const unsigned g = lane >> 4, q = (lane & 15) >> 2, p = lane & 3;
  {
    const unsigned fo = lane < 4 * NT ? WS_FLAG + ((tau0 + (lane >> 2)) * 16 + 4 * kq + (lane & 3)) * 8 : OOB;
    for (unsigned spins = 0; spins < SPIN_CAP; ++spins) {
      const u32x2 f = __builtin_amdgcn_raw_buffer_load_b64(rws, fo, 0, 16);
      const bool ok = lane >= 4 * NT || (f[0] == elo && f[1] == ehi);
      if (__all(ok)) break;
      __builtin_amdgcn_s_sleep(1);
      asm volatile("" ::: "memory");
    }
    asm volatile("" ::: "memory");
  }
  s16x8 of[NT][9];
#pragma unroll
  for (int ks = 0; ks < 9; ++ks)
#pragma unroll
    for (int tt = 0; tt < NT; ++tt)
      of[tt][ks] = __builtin_bit_cast(s16x8, __builtin_amdgcn_raw_buffer_load_b128(
          rws, lane * 16 + ks * 1024, WS_O + (unsigned)(((tau0 + tt) * 36 + kq * 9) * 1024), 16));
  const unsigned sw = 2 * (2 * (g & 1) + (q >> 1));
  unsigned aw[4];
#pragma unroll
  for (int n = 0; n < 4; ++n) aw[n] = (4 * g + q) * 128 + (((2 * (n ^ kq) + (p >> 1)) ^ sw) * 16) + 8 * (p & 1);
#pragma unroll
  for (int ks = 0; ks < 9; ++ks) {
    const unsigned base = ks < 8 ? (unsigned)(kq * 8 + ks) * 4096u : (unsigned)(32 + kq) * 4096u;
#pragma unroll
    for (int n = 0; n < 4; ++n) {
      const s16x8 wf = trd2(L, base + aw[n], base + aw[n] + 2048);
#pragma unroll
      for (int tt = 0; tt < NT; ++tt) acc[tt][n] = mfma32(wf, of[tt][ks], acc[tt][n]);
    }
  }
}

__global__ __launch_bounds__(512, 2) void hedgehog_kernel(const void* x, const void* wA, const void* phiA, const void* woA,
                                                          const void* wB, const void* phiB, const void* woB,
                                                          const void* gate, void* out, void* ws) {
  extern __shared__ __attribute__((aligned(1024))) char lds_raw[];
  LAS char* L = (LAS char*)lds_raw;
  const unsigned LB = (unsigned)(size_t)L;
  const unsigned tid = threadIdx.x, lane = tid & 63;
  const int w = __builtin_amdgcn_readfirstlane(tid >> 6);
  const int bid = blockIdx.x, xcd = bid & 7, r = bid >> 3;
  const int tl = r & 7, gl = r >> 3;
  const int t = (xcd & 1) * 8 + tl, grp = (xcd >> 1) * 4 + gl;
  const int br = grp >> 3, gg = grp & 7;
  const int rot = (tl + gl * 2 + (gl >> 1)) % 9;
  const unsigned g = lane >> 4, q = (lane & 15) >> 2, p = lane & 3, c16 = lane & 15;
  const int tg = w >> 1, hp = w & 1;

  const rsrc_t rx = __builtin_amdgcn_make_buffer_rsrc((void*)x, 0, 2048 * 1152, 0x00020000);
  const rsrc_t rw = __builtin_amdgcn_make_buffer_rsrc((void*)(br ? wB : wA), 0, 576 * 3456, 0x00020000);
  const rsrc_t rws = __builtin_amdgcn_make_buffer_rsrc(ws, 0, WS_BYTES, 0x00020000);

  if (w == 0) {
    const rsrc_t rp = __builtin_amdgcn_make_buffer_rsrc((void*)(br ? phiB : phiA), 0, 32 * 648, 0x00020000);
    dma16(rp, lane * 16, gg * 2592, LB + L_WPHI);
    dma16(rp, lane * 16, gg * 2592 + 1024, LB + L_WPHI + 1024);
    if (lane < 34) dma16(rp, lane * 16, gg * 2592 + 2048, LB + L_WPHI + 2048);
  } else if (w == 1) {
    const rsrc_t rg = __builtin_amdgcn_make_buffer_rsrc((void*)gate, 0, 1152, 0x00020000);
    dma16(rg, lane * 16, 0, LB + L_GATE);
    if (lane < 8) dma16(rg, lane * 16, 1024, LB + L_GATE + 1024);
  } else if (w == 2) {
    if (lane < 1) dma16(rws, lane * 16, WS_EP, LB + L_EP);
  }

  unsigned vx[2], vw[3];
#pragma unroll
  for (int e = 0; e < 2; ++e) {
    const unsigned row = 16 * w + 8 * e + (lane >> 3);
    vx[e] = row * 1152 + (((lane & 7) ^ ((4 * e + (lane >> 4)) & 7)) << 4);
  }
#pragma unroll
  for (int i = 0; i < 3; ++i) {
    const unsigned c = 64 * i + lane, row = c / 18, j = c - row * 18, hi = j >= 9;
    vw[i] = (8 * w + row) * 3456 + (1 + hi) * 1152 + gg * 144 + (j - 9 * hi) * 16;
  }
  auto stage_of = [&](int it) { int s = it + rot; return s >= 9 ? s - 9 : s; };
  auto issue_stage = [&](int it) {
    const int st = stage_of(it);
    const unsigned sl = LB + ((it + 3) & 3) * SLOT;
    const unsigned sx = t * 147456 + st * 128, sww = st * (64 * 3456);
    dma16(rx, vx[0], sx, sl + (2 * w) * 1024);
    dma16(rx, vx[1], sx, sl + (2 * w + 1) * 1024);
    dma16(rw, vw[0], sww, sl + XB + w * WBLK);
    dma16(rw, vw[1], sww, sl + XB + w * WBLK + 1024);
    if (lane < 16) dma16(rw, vw[2], sww, sl + XB + w * WBLK + 2048);
  };
  issue_stage(0); issue_stage(1); issue_stage(2);

  auto wq_voff = [&](unsigned np) {
    const unsigned c = 64 * np + lane, blk = c / 72, cc = c - blk * 72, rp_ = cc / 9, jj = cc - rp_ * 9;
    return (blk * 8 + 4 * (rp_ & 1) + (rp_ >> 1)) * 3456 + gg * 144 + jj * 16;
  };
  const unsigned vq0 = wq_voff(w), vq8 = wq_voff(8);
  auto issue_wq = [&](int it) { dma16(rw, vq0, stage_of(it) * (64 * 3456), LB + it * 9216 + w * 1024); };

  const unsigned tokA = 32 * tg + 16 * hp + c16, tokB = tokA ^ 16;
  const unsigned axA = tokA * 128 + ((g ^ (c16 >> 1)) << 4), axB = axA ^ 2048;
  const unsigned aw0 = XB + g * WBLK + q * 288 + (9 * hp + p) * 8;
  const unsigned u2 = (int)p == hp ? 9 * hp + 8 : (int)p == (hp ^ 1) ? 9 * hp + 26 : p == 2 ? 9 * hp + 5 : 9 * hp + 7;
  const unsigned aw2 = XB + g * WBLK + q * 288 + u2 * 8;

  f32x4 acc[2][5];
#pragma unroll
  for (int a = 0; a < 2; ++a)
#pragma unroll
    for (int m = 0; m < 5; ++m) acc[a][m] = f32x4{0.f, 0.f, 0.f, 0.f};
  s16x8 xk[18];

#pragma unroll
  for (int it = 0; it < 9; ++it) {
    if (it <= 6) wait_bar<10>(); else if (it == 7) wait_bar<8>(); else wait_bar<7>();
    if (it + 3 <= 8) issue_stage(it + 3);
    else if (it == 6) { issue_wq(0); issue_wq(1); issue_wq(2); }
    else if (it == 7) { issue_wq(3); issue_wq(4); issue_wq(5); issue_wq(6); }
    else {
      issue_wq(7); issue_wq(8);
      dma16(rw, vq8, stage_of(w) * (64 * 3456), LB + w * 9216 + 8192);
      if (w == 0) dma16(rw, vq8, stage_of(8) * (64 * 3456), LB + 8 * 9216 + 8192);
    }
    if (it == 0) {
      LAS const unsigned short* wp = (LAS const unsigned short*)(L + L_WPHI);
#pragma unroll
      for (int k = 0; k < 3; ++k) {
        const unsigned n = tid + 512 * k, fr = n >> 8, hp_ = fr / 3, u = fr - 3 * hp_, ln = (n >> 2) & 63, jp = n & 3;
        const unsigned gp = (ln & 15) >> 2, i = ln & 3, gk = ln >> 4, idx = 4 * u + i, hh = gp >> 1, f = 9 * (gp & 1) + idx;
        const int c0 = (jp < 2 ? 4 * gk + 2 * jp : 16 + 4 * gk + 2 * (jp - 2)) - 18 * (int)hh;
        const bool ok = idx < 9 && c0 >= 0 && c0 < 18;
        const unsigned a = ok ? (2 * hp_ + hh) * 324 + c0 * 18 + f : 0;
        const unsigned v = (unsigned)wp[a] | ((unsigned)wp[a + 18] << 16);
        *(LAS unsigned*)(L + L_A0 + n * 4) = ok ? v : 0u;
      }
#pragma unroll
      for (int k = 0; k < 2; ++k) {
        const unsigned n = tid + 512 * k, fr = n >> 7, hp_ = fr / 3, u = fr - 3 * hp_, ln = (n >> 1) & 63, jp = n & 1;
        const unsigned gp = (ln & 15) >> 2, i = ln & 3, gk = ln >> 4, idx = 4 * u + i, hh = gp >> 1, f = 9 * (gp & 1) + idx;
        const bool ok = idx < 9 && gk == hp_ && hh == 1 && n < 768;
        const unsigned a = ok ? (2 * hp_ + 1) * 324 + (14 + 2 * jp) * 18 + f : 0;
        const unsigned v = (unsigned)wp[a] | ((unsigned)wp[a + 18] << 16);
        if (n < 768) *(LAS unsigned*)(L + L_A1 + n * 4) = ok ? v : 0u;
      }
    }
    if (it == 1) {
      if (tid < 480) *(LAS u32x4*)(L + L_KVT + tid * 16) = u32x4{0u, 0u, 0u, 0u};
    }
    const unsigned sl = ((it + 3) & 3) * SLOT;
#pragma unroll
    for (int ks = 0; ks < 2; ++ks) {
      const s16x8 xa = rd128(L, sl + (axA ^ (ks * 64)));
      const s16x8 xb = rd128(L, sl + (axB ^ (ks * 64)));
      xk[2 * it + ks] = xa;
      const unsigned b0 = sl + aw0 + ks * (4 * WBLK), b2 = sl + aw2 + ks * (4 * WBLK);
      s16x8 wf[5];
      wf[0] = trd2(L, b0, b0 + 1152);
      wf[1] = trd2(L, b0 + 32, b0 + 32 + 1152);
      wf[2] = trd2(L, b2, b2 + 1152);
      wf[3] = trd2(L, b0 + 144, b0 + 144 + 1152);
      wf[4] = trd2(L, b0 + 176, b0 + 176 + 1152);
#pragma unroll
      for (int m = 0; m < 5; ++m) {
        acc[0][m] = mfma32(wf[m], xa, acc[0][m]);
        acc[1][m] = mfma32(wf[m], xb, acc[1][m]);
      }
    }
  }

  unsigned elo, ehi;
  {
    const u32x2 e = *(LAS u32x2*)(L + L_EP);
    const unsigned long long ev = (((unsigned long long)e[1] << 32) | e[0]) + 1ull;
    elo = __builtin_amdgcn_readfirstlane((unsigned)ev);
    ehi = __builtin_amdgcn_readfirstlane((unsigned)(ev >> 32));
  }
  const unsigned tlo = elo << 24, thi = (elo >> 8) | (ehi << 24);

  {
    unsigned ph[2][9];
#pragma unroll
    for (int a = 0; a < 2; ++a) {
      f32x4 pk[3];
      transform(L, hp, lane, acc[a][0], acc[a][1], acc[a][2], pk);
      featmap(pk[0], pk[1], pk[2], ph[a]);
    }
    bar();
    const unsigned hl = 2 * hp + (g >> 1), half = g & 1;
#pragma unroll
    for (int a = 0; a < 2; ++a) {
      const unsigned tok = a ? tokB : tokA;
#pragma unroll
      for (int e = 0; e < 4; ++e)
        *(LAS u32x2*)(L + L_PHI + (9 * hl + 4 * half + e) * 1024 + ((tok ^ (8 * ((hl + e) & 3))) << 3)) = u32x2{ph[a][2 * e], ph[a][2 * e + 1]};
      *(LAS unsigned*)(L + L_PHI + (9 * hl + 8) * 1024 + ((tok ^ (8 * (hl & 3))) << 3) + 4 * half) = ph[a][8];
      const unsigned vp = 9 * hp + g;
      *(LAS u32x2*)(L + L_V + vp * 1024 + ((tok ^ (8 * (vp & 3))) << 3)) = u32x2{pk2(acc[a][3][0], acc[a][3][1]), pk2(acc[a][3][2], acc[a][3][3])};
      *(LAS u32x2*)(L + L_V + (vp + 4) * 1024 + ((tok ^ (8 * (vp & 3))) << 3)) = u32x2{pk2(acc[a][4][0], acc[a][4][1]), pk2(acc[a][4][2], acc[a][4][3])};
      if ((int)g == (hp ^ 1))
        *(LAS u32x2*)(L + L_V + (9 * hp + 8) * 1024 + ((tok ^ (8 * (hp & 3))) << 3)) = u32x2{pk2(acc[a][2][0], acc[a][2][1]), pk2(acc[a][2][2], acc[a][2][3])};
    }
  }
  bar();
  {
    const int hl = w >> 1, nt = w & 1;
    f32x4 kv[3];
#pragma unroll
    for (int m = 0; m < 3; ++m) kv[m] = f32x4{0.f, 0.f, 0.f, 0.f};
    const unsigned vp = 9 * (hl >> 1) + (nt ? 4 : (hl & 1) ? 5 : 0) + p;
    const unsigned tk = 4 * g + q;
    const unsigned av = L_V + vp * 1024 + ((tk ^ (8 * (vp & 3))) << 3);
    const unsigned ap = L_PHI + (9 * hl + p) * 1024 + ((tk ^ (8 * ((hl + p) & 3))) << 3);
    const bool ones = nt && c16 == 4;
#pragma unroll
    for (int kk = 0; kk < 4; ++kk) {
      s16x8 vb = trd2(L, av + kk * 256, (av ^ 128) + kk * 256);
      if (ones) vb = mk8(0x3f803f80u, 0x3f803f80u, 0x3f803f80u, 0x3f803f80u);
#pragma unroll
      for (int m = 0; m < 3; ++m) {
        const s16x8 pa = trd2(L, ap + m * 4096 + kk * 256, (ap ^ 128) + m * 4096 + kk * 256);
        kv[m] = mfma32(pa, vb, kv[m]);
      }
    }
    const int dd = c16 == 4 ? 2 : (int)c16 - 2 * (hl & 1);
    const bool cok = nt == 0 || c16 == 4 || (dd >= 0 && dd < 2);
#pragma unroll
    for (int m = 0; m < 3; ++m)
#pragma unroll
      for (int h = 0; h < 2; ++h) {
        const bool ok = cok && (m < 2 || g == 0);
        const unsigned uu = nt ? 288 + ((4 * m + g) * 2 + h) * 3 + dd : (m < 2 ? (m * 2 + h) * 64 + lane : 256 + h * 16 + c16);
        const unsigned u = hl * 342 + uu, j = u / SLICE, off = u - j * SLICE;
        const unsigned vo = ok ? WS_H1 + (((grp * 16 + j) * 16 + t) * SLICE + off) * 16 : OOB;
        const u32x4 gv = {g24(kv[m][2 * h], tlo), thi, g24(kv[m][2 * h + 1], tlo), thi};
        __builtin_amdgcn_raw_buffer_store_b128(gv, rws, vo, 0, 16);
      }
  }

  const int idx3 = (234 * xcd + 7) / 8 + r;
  const bool act3 = idx3 < (234 * (xcd + 1) + 7) / 8;
  const int rt = idx3 / 9, ct = idx3 - rt * 9;
  const rsrc_t roA = __builtin_amdgcn_make_buffer_rsrc((void*)woA, 0, 576 * 1152, 0x00020000);
  const rsrc_t roB = __builtin_amdgcn_make_buffer_rsrc((void*)woB, 0, 576 * 1152, 0x00020000);
  unsigned vo0, vo1;
  {
    const unsigned s = w >> 2, e = w & 3, tt_ = e >> 1, gq = 2 * (e & 1) + (lane >> 5), qq = (lane >> 3) & 3, j = 4 * tt_ + qq;
    const unsigned ch = ((lane & 7) ^ (2 * ((lane >> 4) & 3))) * 16 + ct * 128;
    const unsigned rr = 8 * s + j;
    vo0 = (9 * gq + rr + (rr >= 9 ? 27 : 0)) * 1152 + ch;
    vo1 = (72 * (j >> 1) + 9 * gq + 43 + (j & 1)) * 1152 + ch;
  }
  auto issue_wo = [&](int pr) {
    if (act3) dma16(pr >= 8 ? roB : roA, vo0, (pr & 7) * (72 * 1152), LB + (2 * pr + (w >> 2)) * 4096 + (w & 3) * 1024);
  };
  auto issue_wo_left = [&](int which) {
    const int kq = 2 * (w >> 2) + which;
    if (act3) { if (w >> 2) dma16(roB, vo1, which * (288 * 1152), LB + (32 + kq) * 4096 + (w & 3) * 1024);
                else        dma16(roA, vo1, which * (288 * 1152), LB + (32 + kq) * 4096 + (w & 3) * 1024); }
  };

  wait_bar<6>();
  issue_wo(11); issue_wo(12); issue_wo(13); issue_wo(14); issue_wo(15); issue_wo_left(0);

  f32x4 qa[6];
#pragma unroll
  for (int m = 0; m < 6; ++m) qa[m] = f32x4{0.f, 0.f, 0.f, 0.f};
  const unsigned aq0 = g * 1152 + q * 288 + p * 8;
  const unsigned aq2 = g * 1152 + q * 288 + (p == 0 ? 8 : p == 1 ? 17 : p == 2 ? 6 : 7) * 8;
  auto qstep = [&](int i) {
    const unsigned b0 = aq0 + i * 4608, b2 = aq2 + i * 4608;
    const int sks = stage_of(i >> 1) * 2 + (i & 1);
    s16x8 wf[6];
    wf[0] = trd2(L, b0, b0 + 144);
    wf[1] = trd2(L, b0 + 32, b0 + 32 + 144);
    wf[2] = trd2(L, b2, b2 + 144);
    wf[3] = trd2(L, b0 + 72, b0 + 72 + 144);
    wf[4] = trd2(L, b0 + 104, b0 + 104 + 144);
    wf[5] = rd128(L, L_GATE + sks * 64 + g * 16);
#pragma unroll
    for (int m = 0; m < 6; ++m) qa[m] = mfma32(wf[m], xk[i], qa[m]);
  };
#pragma unroll
  for (int i = 0; i < 10; ++i) qstep(i);

  {
    const unsigned ul = SLICE * t + 11 * w + c16;
    const bool val = c16 < 11 && ul < NUNITS;
    const unsigned vo = val ? WS_H1 + (((grp * 16 + t) * 16 + 4 * g) * SLICE + 11 * w + c16) * 16 : OOB;
    u32x4 gv[4];
    for (unsigned spins = 0; spins < SPIN_CAP; ++spins) {
      bool ok = true;
#pragma unroll
      for (int k = 0; k < 4; ++k) gv[k] = __builtin_amdgcn_raw_buffer_load_b128(rws, vo, k * (SLICE * 16), 16);
#pragma unroll
      for (int k = 0; k < 4; ++k) ok = ok && gok(gv[k], tlo, thi);
      if (__all(ok || !val)) break;
      __builtin_amdgcn_s_sleep(1);
      asm volatile("" ::: "memory");
    }
    float s0 = (ung24(gv[0][0]) + ung24(gv[1][0])) + (ung24(gv[2][0]) + ung24(gv[3][0]));
    float s1 = (ung24(gv[0][2]) + ung24(gv[1][2])) + (ung24(gv[2][2]) + ung24(gv[3][2]));
    { F2 a = sw16(s0); s0 = a.a + a.b; F2 b = sw16(s1); s1 = b.a + b.b; }
    { F2 a = sw32(s0); s0 = a.a + a.b; F2 b = sw32(s1); s1 = b.a + b.b; }
    const u32x4 o = {g24(s0, tlo), thi, g24(s1, tlo), thi};
    __builtin_amdgcn_raw_buffer_store_b128(o, rws, (val && g == 0) ? WS_H2 + (grp * 1408 + ul) * 16 : OOB, 0, 16);
  }
  bar();
  issue_wo(0); issue_wo(1); issue_wo(2); issue_wo(3); issue_wo(4);
#pragma unroll
  for (int i = 10; i < 18; ++i) qstep(i);
  bar();
  issue_wo(5); issue_wo(6); issue_wo(7); issue_wo(8); issue_wo(9); issue_wo(10);

  unsigned pq[2][9];
  {
    f32x4 pp[3];
    transform(L, 0, lane, qa[0], qa[1], qa[2], pp);
    featmap(pp[0], pp[1], pp[2], pq[0]);
    transform(L, 1, lane, qa[3], qa[4], qa[2], pp);
    featmap(pp[0], pp[1], pp[2], pq[1]);
  }
  float gt = __builtin_amdgcn_rcpf(1.f + __builtin_amdgcn_exp2f(qa[5][0] * (br ? 1.44269504088896340736f : -1.44269504088896340736f)));

  {
    unsigned vo[3], la[3];
#pragma unroll
    for (int k = 0; k < 3; ++k) {
      const unsigned u = tid + 512 * k;
      const bool val = u < NUNITS;
      vo[k] = val ? WS_H2 + (grp * 1408 + u) * 16 : OOB;
      const unsigned hl = u / 342, uu = u - hl * 342, odd = hl & 1;
      unsigned piece, h, ds;
      if (uu < 256) { piece = 4 * (uu >> 7) + ((uu >> 4) & 3); h = (uu >> 6) & 1; ds = (uu & 15) + 2 * odd; }
      else if (uu < 288) { piece = 8; h = (uu >> 4) & 1; ds = (uu & 15) + 2 * odd; }
      else { const unsigned v = uu - 288, phh = v / 3, dd = v - 3 * phh; piece = phh >> 1; h = phh & 1; ds = dd == 2 ? 18 : (odd ? dd : 16 + dd); }
      const unsigned pos = piece < 8 ? (piece >> 2) * 24 + 4 * (piece & 3) + 2 * h : h * 24 + 16;
      la[k] = L_KVT + ((hl * 20 + ds) * 48 + pos) * 2;
    }
    u32x4 gv[3];
    for (unsigned spins = 0; spins < SPIN_CAP; ++spins) {
      bool ok = true;
#pragma unroll
      for (int k = 0; k < 3; ++k) gv[k] = __builtin_amdgcn_raw_buffer_load_b128(rws, vo[k], 0, 16);
#pragma unroll
      for (int k = 0; k < 3; ++k) ok = ok && (vo[k] == OOB || gok(gv[k], tlo, thi));
      if (__all(ok)) break;
      __builtin_amdgcn_s_sleep(1);
      asm volatile("" ::: "memory");
    }
#pragma unroll
    for (int k = 0; k < 3; ++k)
      if (vo[k] != OOB) *(LAS unsigned*)(L + la[k]) = pk2(ung24(gv[k][0]), ung24(gv[k][2]));
  }
  bar();
  issue_wo_left(1);

  {
    const unsigned gp = c16 >> 2, i = c16 & 3;
    float o[18];
#pragma unroll
    for (int pr = 0; pr < 2; ++pr) {
      f32x4 nm[3];
#pragma unroll
      for (int u = 0; u < 3; ++u) {
        const unsigned idx = 4 * u + i;
        const bool ok = (g >> 1) == (gp >> 1) && idx <= 9;
        const unsigned ds = !ok ? 19 : idx < 9 ? 9 * (gp & 1) + idx : 18;
        const unsigned a = L_KVT + (((2 * pr + (gp >> 1)) * 20 + ds) * 48 + (g & 1) * 24) * 2;
        f32x4 z = {0.f, 0.f, 0.f, 0.f};
        z = mfma32(rd128(L, a), mk8(pq[pr][0], pq[pr][1], pq[pr][2], pq[pr][3]), z);
        z = mfma32(rd128(L, a + 16), mk8(pq[pr][4], pq[pr][5], pq[pr][6], pq[pr][7]), z);
        nm[u] = mfma32(rd128(L, a + 32), mk8(pq[pr][8], 0u, 0u, 0u), z);
      }
      const float sc = gt * __builtin_amdgcn_rcpf(nm[2][1] + 1e-6f);
#pragma unroll
      for (int k = 0; k < 9; ++k) o[9 * pr + k] = nm[k >> 2][k & 3] * sc;
    }
    const int tau = 8 * t + 2 * tg + hp, kq = grp >> 2, pp = grp & 3;
    const unsigned ob = WS_O + (unsigned)((tau * 36 + kq * 9) * 1024) + lane * 16;
    __builtin_amdgcn_raw_buffer_store_b128(u32x4{pk2(o[0], o[1]), pk2(o[2], o[3]), pk2(o[4], o[5]), pk2(o[6], o[7])}, rws, ob + pp * 2048, 0, 16);
    __builtin_amdgcn_raw_buffer_store_b128(u32x4{pk2(o[8], o[9]), pk2(o[10], o[11]), pk2(o[12], o[13]), pk2(o[14], o[15])}, rws, ob + pp * 2048 + 1024, 0, 16);
    __builtin_amdgcn_raw_buffer_store_b32(pk2(o[16], o[17]), rws, ob + 8192 + pp * 4, 0, 16);
    drain();
    __builtin_amdgcn_raw_buffer_store_b64(u32x2{elo, ehi}, rws, lane == 0 ? WS_FLAG + (tau * 16 + grp) * 8 : OOB, 0, 16);
  }
  if (!act3) return;
  bar();

  {
    const int kq = w & 3, th = w >> 2;
    const int tau0 = 5 * rt + 3 * th;
    const int nt = th == 0 ? 3 : (rt < 25 ? 2 : 0);
    f32x4 a3[3][4];
#pragma unroll
    for (int a = 0; a < 3; ++a)
#pragma unroll
      for (int n = 0; n < 4; ++n) a3[a][n] = f32x4{0.f, 0.f, 0.f, 0.f};
    if (nt == 3) p3_body<3>(L, rws, lane, kq, tau0, elo, ehi, a3);
    else if (nt == 2) p3_body<2>(L, rws, lane, kq, tau0, elo, ehi, a3);
    bar();
#pragma unroll
    for (int a = 0; a < 3; ++a)
      if (a < nt)
#pragma unroll
        for (int n = 1; n < 4; ++n)
          *(LAS f32x4*)(L + (((n ^ kq) * 3 + (n - 1)) * 5 + 3 * th + a) * 1024 + lane * 16) = a3[a][n];
    bar();
    const rsrc_t rout = __builtin_amdgcn_make_buffer_rsrc(out, 0, 2048 * 1152, 0x00020000);
#pragma unroll
    for (int a = 0; a < 3; ++a)
      if (a < nt) {
        f32x4 s = a3[a][0];
#pragma unroll
        for (int n = 0; n < 3; ++n) s += *(LAS f32x4*)(L + ((kq * 3 + n) * 5 + 3 * th + a) * 1024 + lane * 16);
        __builtin_amdgcn_raw_buffer_store_b64(u32x2{pk2(s[0], s[1]), pk2(s[2], s[3])}, rout,
                                              ((tau0 + a) * 16 + c16) * 1152 + ct * 128 + kq * 32 + g * 8, 0, 0);
      }
    if (bid == 0 && tid == 0) __builtin_amdgcn_raw_buffer_store_b64(u32x2{elo, ehi}, rws, WS_EP, 0, 16);
  }
}

extern "C" void kernel_launch(void* const* d_in, const int* in_sizes, int n_in, void* d_out, int out_size, void* d_ws,
                              size_t ws_size, hipStream_t stream) {
  (void)hipFuncSetAttribute((const void*)hedgehog_kernel, hipFuncAttributeMaxDynamicSharedMemorySize, LDS_BYTES);
  hedgehog_kernel<<<256, 512, LDS_BYTES, stream>>>(d_in[0], d_in[1], d_in[2], d_in[3], d_in[5], d_in[6], d_in[7], d_in[8],
                                                   d_out, d_ws);
}
```

```cpp
#include <hip/hip_runtime.h>
#include <stdint.h>

typedef short s16x4 __attribute__((ext_vector_type(4)));
typedef short s16x8 __attribute__((ext_vector_type(8)));
typedef float f32x2 __attribute__((ext_vector_type(2)));
typedef float f32x4 __attribute__((ext_vector_type(4)));
typedef float f32x16 __attribute__((ext_vector_type(16)));
typedef unsigned u32x4 __attribute__((ext_vector_type(4)));
typedef unsigned u32x2 __attribute__((ext_vector_type(2)));
typedef __bf16 bf16x2_t __attribute__((ext_vector_type(2)));
typedef __amdgpu_buffer_rsrc_t rsrc_t;
#define LAS __attribute__((address_space(3)))
#define DI __device__ __forceinline__

constexpr unsigned SLOT = 34816, XB = 16384;
constexpr unsigned L_PHI = 82944, L_V = L_PHI + 36 * 1024;
constexpr unsigned L_TMP = 2 * SLOT;
constexpr unsigned L_AF = 139264, L_GATE = L_AF + 11 * 1024;
constexpr unsigned L_ONES = L_GATE + 4608, L_KA = L_ONES + 1024, L_KB = L_KA + 5120, L_KZ = L_KB + 1728, L_CNT = L_KZ + 160;
constexpr unsigned LDS_BYTES = L_CNT + 64 + 256;
static_assert((L_CNT + 64 - L_KA) % 32 == 0 && (L_CNT + 64 - L_KA) / 32 <= 256, "zero fill");
constexpr unsigned WS_EP = 0, WS_FLAG = 4096, WS_H2 = 32768, WS_H1 = 1u << 18, WS_O = WS_H1 + 256 * 12288, WS_BYTES = 16u << 20;
constexpr unsigned OOB = 0x80000000u;
constexpr unsigned C_RING = L_CNT, C_PHI = L_CNT + 4, C_WQA = L_CNT + 8, C_WQB = L_CNT + 12, C_KF = L_CNT + 16, C_FREE = L_CNT + 20, C_KVT = L_CNT + 24, C_KD = L_CNT + 28, L_SINK = L_CNT + 32;
constexpr unsigned SPIN_CAP = 1u << 18;
constexpr int D1 = 8, D2 = 8, HOPSLEEP = 1, FLAGSLEEP = 2;
constexpr int OPD = 2, WD3 = 14, WD2 = 24;

DI void dma16(rsrc_t rs, unsigned voff, unsigned soff, unsigned ldsaddr) {
  asm volatile("s_mov_b32 m0, %0\n\ts_nop 0\n\tbuffer_load_dwordx4 %1, %2, %3 offen lds"
               :: "s"(ldsaddr), "v"(voff), "s"(rs), "s"(soff) : "memory");
}
DI void touch(rsrc_t rs, unsigned voff, unsigned soff) {
  asm volatile("s_mov_b32 m0, %0\n\ts_nop 0\n\tbuffer_load_dword %1, %2, %3 offen sc1 lds"
               :: "s"(L_CNT + 64), "v"(voff), "s"(rs), "s"(soff) : "memory");
}
DI void dma16m(rsrc_t rs, unsigned voff, unsigned soff, unsigned ldsaddr, unsigned long long mask) {
  asm volatile("s_mov_b32 m0, %0\n\ts_mov_b64 exec, %4\n\tbuffer_load_dwordx4 %1, %2, %3 offen lds\n\ts_mov_b64 exec, -1"
               :: "s"(ldsaddr), "v"(voff), "s"(rs), "s"(soff), "s"(mask) : "memory");
}
template <int N> DI void wait_bar() { asm volatile("s_waitcnt vmcnt(%0) lgkmcnt(0)\n\ts_barrier" :: "i"(N) : "memory"); }
DI void bar() { asm volatile("s_waitcnt lgkmcnt(0)\n\ts_barrier" ::: "memory"); }
DI void drain() { asm volatile("s_waitcnt vmcnt(0)" ::: "memory"); }
#define KEEP(x) asm volatile("" : "+v"(x))
#define SB() __builtin_amdgcn_sched_barrier(0)
template <int V> struct IC { static constexpr int value = V; };
template <int B, int E, class F> DI void sfor(F&& f) { if constexpr (B < E) { f(IC<B>{}); sfor<B + 1, E>(f); } }
#define LAM __attribute__((always_inline))

DI unsigned pk2(float a, float b) { f32x2 v = {a, b}; return __builtin_bit_cast(unsigned, __builtin_convertvector(v, bf16x2_t)); }
DI unsigned pk2(f32x2 v) { return __builtin_bit_cast(unsigned, __builtin_convertvector(v, bf16x2_t)); }
DI s16x8 mk8(unsigned a, unsigned b, unsigned c, unsigned d) { u32x4 v = {a, b, c, d}; return __builtin_bit_cast(s16x8, v); }
DI f32x4 mfma16(s16x8 a, s16x8 b, f32x4 c) { return __builtin_amdgcn_mfma_f32_16x16x32_bf16(a, b, c, 0, 0, 0); }
DI f32x16 mfma32(s16x8 a, s16x8 b, f32x16 c) { return __builtin_amdgcn_mfma_f32_32x32x16_bf16(a, b, c, 0, 0, 0); }
#define LP(T, a) ((LAS T*)(size_t)(unsigned)(a))
DI s16x4 trd(unsigned a) { return __builtin_amdgcn_ds_read_tr16_b64_v4i16(LP(s16x4, a)); }
DI s16x8 trd2(unsigned a0, unsigned a1) {
  s16x4 lo = trd(a0), hi = trd(a1);
  return __builtin_shufflevector(lo, hi, 0, 1, 2, 3, 4, 5, 6, 7);
}
DI s16x8 rd128(unsigned a) { return *LP(s16x8, a); }
struct F2 { float a, b; };
DI F2 sw16(float x) { auto r = __builtin_amdgcn_permlane16_swap(__float_as_uint(x), __float_as_uint(x), false, false);
  return F2{__uint_as_float(r[0]), __uint_as_float(r[1])}; }
DI F2 sw32(float x) { auto r = __builtin_amdgcn_permlane32_swap(__float_as_uint(x), __float_as_uint(x), false, false);
  return F2{__uint_as_float(r[0]), __uint_as_float(r[1])}; }
DI void lds_signal(unsigned addr) {
  unsigned one = 1;
  asm volatile("s_waitcnt lgkmcnt(0)\n\ts_mov_b64 exec, 1\n\tds_add_u32 %0, %1\n\ts_mov_b64 exec, -1" :: "v"(addr), "v"(one) : "memory");
}
template <int N> DI void lds_wait(unsigned addr) {
  unsigned tv, ts, cnt = 1u << 22;
  asm volatile("1:\n\tds_read_b32 %0, %3\n\ts_sub_u32 %2, %2, 1\n\ts_cbranch_scc1 2f\n\ts_waitcnt lgkmcnt(0)\n\tv_readfirstlane_b32 %1, %0\n\t"
               "s_cmp_ge_u32 %1, %4\n\ts_cbranch_scc1 2f\n\ts_sleep 1\n\ts_branch 1b\n2:\n\ts_waitcnt lgkmcnt(0)"
               : "=&v"(tv), "=&s"(ts), "+s"(cnt) : "v"(addr), "i"(N) : "memory", "scc");
}
DI float addf(float a, float b) { float r; asm("v_add_f32 %0, %1, %2" : "=v"(r) : "v"(a), "v"(b)); return r; }
DI float mx3(float a, float b, float c) { return __builtin_fmaxf(__builtin_fmaxf(a, b), c); }
DI float mn3(float a, float b, float c) { return __builtin_fminf(__builtin_fminf(a, b), c); }

#define OPQ(x) asm("" : "+v"(x))
template <bool PK, bool RAW = false, class H>
DI float featmap(const f32x16 p, float e0, float e1, unsigned (&ph)[18], H&& hook) {
  const float mx = __builtin_fmaxf(mx3(mx3(p[0], p[1], p[2]), mx3(p[3], p[4], p[5]), mx3(p[6], p[7], p[8])),
                                   mx3(mx3(p[9], p[10], p[11]), mx3(p[12], p[13], p[14]), mx3(p[15], e0, e1)));
  const float mn = __builtin_fminf(mn3(mn3(p[0], p[1], p[2]), mn3(p[3], p[4], p[5]), mn3(p[6], p[7], p[8])),
                                   mn3(mn3(p[9], p[10], p[11]), mn3(p[12], p[13], p[14]), mn3(p[15], e0, e1)));
  const float c = 1.44269504088896340736f;
  if constexpr (PK) {
    const f32x2 cp = {c, c}, cn = {-c, -c}, bp = {-mx * c, -mx * c}, bn = {mn * c, mn * c};
    f32x2 ep[9], en[9];
    sfor<0, 9>([&](auto I_) LAM {
      constexpr int i = decltype(I_)::value;
      const f32x2 v = i < 8 ? f32x2{p[2 * (i & 7)], p[2 * (i & 7) + 1]} : f32x2{e0, e1};
      const f32x2 a = __builtin_elementwise_fma(v, cp, bp), b = __builtin_elementwise_fma(v, cn, bn);
      ep[i] = f32x2{__builtin_amdgcn_exp2f(a[0]), __builtin_amdgcn_exp2f(a[1])};
      en[i] = f32x2{__builtin_amdgcn_exp2f(b[0]), __builtin_amdgcn_exp2f(b[1])};
      hook(I_);
    });
    const f32x2 sp = ((ep[0] + ep[1]) + (ep[2] + ep[3])) + ((ep[4] + ep[5]) + (ep[6] + ep[7])) + ep[8];
    const f32x2 sn = ((en[0] + en[1]) + (en[2] + en[3])) + ((en[4] + en[5]) + (en[6] + en[7])) + en[8];
    const float s1 = sp[0] + sp[1], rn = __builtin_amdgcn_rcpf(sn[0] + sn[1]);
    if constexpr (RAW) {
      const f32x2 rn2 = {rn * s1, rn * s1};
#pragma unroll
      for (int i = 0; i < 9; ++i) { ph[i] = pk2(ep[i]); ph[9 + i] = pk2(en[i] * rn2); }
    } else {
      const float rp = __builtin_amdgcn_rcpf(s1);
      const f32x2 rp2 = {rp, rp}, rn2 = {rn, rn};
#pragma unroll
      for (int i = 0; i < 9; ++i) { ph[i] = pk2(ep[i] * rp2); ph[9 + i] = pk2(en[i] * rn2); }
    }
    return s1;
  } else {
    float bp = -mx * c, bn = mn * c, cv = c;
    OPQ(bp); OPQ(bn); OPQ(cv);
    float ep[18], en[18], sp = 0.f, sn = 0.f;
    sfor<0, 9>([&](auto I_) LAM {
      constexpr int i = decltype(I_)::value;
#pragma unroll
      for (int k = 0; k < 2; ++k) {
        const float v = i < 8 ? p[2 * (i & 7) + k] : (k ? e1 : e0);
        float a = __builtin_fmaf(v, cv, bp), b = __builtin_fmaf(-v, cv, bn);
        OPQ(a); OPQ(b);
        ep[2 * i + k] = __builtin_amdgcn_exp2f(a); en[2 * i + k] = __builtin_amdgcn_exp2f(b);
        sp += ep[2 * i + k]; OPQ(sp);
        sn += en[2 * i + k]; OPQ(sn);
      }
      hook(I_);
    });
    const float rp = __builtin_amdgcn_rcpf(sp), rn = __builtin_amdgcn_rcpf(sn);
#pragma unroll
    for (int i = 0; i < 18; ++i) { ep[i] *= rp; OPQ(ep[i]); en[i] *= rn; OPQ(en[i]); }
#pragma unroll
    for (int i = 0; i < 9; ++i) { ph[i] = pk2(ep[2 * i], ep[2 * i + 1]); ph[9 + i] = pk2(en[2 * i], en[2 * i + 1]); }
    return sp;
  }
}
DI void transform(const s16x8 (&A)[11], const f32x16 t0, const f32x16 t1, unsigned l0, unsigned l1, f32x16& pa, f32x16& pb, f32x16& pl) {
  s16x8 b[5];
  b[0] = mk8(pk2(t0[0], t0[1]), pk2(t0[2], t0[3]), pk2(t0[4], t0[5]), pk2(t0[6], t0[7]));
  b[1] = mk8(pk2(t0[8], t0[9]), pk2(t0[10], t0[11]), pk2(t0[12], t0[13]), pk2(t0[14], t0[15]));
  b[2] = mk8(pk2(t1[0], t1[1]), pk2(t1[2], t1[3]), pk2(t1[4], t1[5]), pk2(t1[6], t1[7]));
  b[3] = mk8(pk2(t1[8], t1[9]), pk2(t1[10], t1[11]), pk2(t1[12], t1[13]), pk2(t1[14], t1[15]));
  b[4] = mk8(l0, l1, 0u, 0u);
  const f32x16 z = {};
  pl = mfma32(A[6], b[0], z);
  pa = mfma32(A[0], b[0], z);
  pl = mfma32(A[7], b[1], pl);
  pa = mfma32(A[1], b[1], pa);
  pl = mfma32(A[8], b[2], pl);
  pa = mfma32(A[2], b[2], pa);
  pl = mfma32(A[9], b[3], pl);
  pl = mfma32(A[10], b[4], pl);
  pb = mfma32(A[3], b[2], z);
  pb = mfma32(A[4], b[3], pb);
  pb = mfma32(A[5], b[4], pb);
}
DI void take_epoch(const u32x2 ev, unsigned& elo, unsigned& ehi) {
  unsigned lo, hi;
  asm volatile("v_readfirstlane_b32 %0, %2\n\tv_readfirstlane_b32 %1, %3\n\ts_nop 4" : "=s"(lo), "=s"(hi) : "v"(ev[0]), "v"(ev[1]) : "memory");
  const unsigned long long e = (((unsigned long long)hi << 32) | lo) + 1ull;
  elo = (unsigned)e; ehi = (unsigned)(e >> 32);
}
DI u32x4 unit(unsigned a, unsigned b, unsigned tag) { return u32x4{a, tag, b, tag}; }
DI float blo(unsigned v) { return __uint_as_float(v << 16); }
DI float bhi(unsigned v) { return __uint_as_float(v & 0xffff0000u); }

template <int NT>
DI void p3_body(rsrc_t rws, unsigned lane, int kq, int tau0, unsigned elo, unsigned ehi, f32x4 (&acc)[3][4]) {
  const unsigned g = lane >> 4, q = (lane & 15) >> 2, p = lane & 3;
  const unsigned sw = 2 * (2 * (g & 1) + (q >> 1));
  unsigned aw[4];
#pragma unroll
  for (int n = 0; n < 4; ++n) { aw[n] = kq * 36864 + (4 * g + q) * 128 + (((2 * (n ^ kq) + (p >> 1)) ^ sw) * 16) + 8 * (p & 1); KEEP(aw[n]); }
  unsigned vo = WS_O + (unsigned)((tau0 * 36 + kq * 9) * 1024) + lane * 16;
  KEEP(vo);
  constexpr int D = NT == 3 ? WD3 : WD2, W = D + 1;
  s16x8 wf[W];
#pragma unroll
  for (int nn = 0; nn < D; ++nn) wf[nn] = trd2(aw[nn & 3] + (nn >> 2) * 4096, aw[nn & 3] + (nn >> 2) * 4096 + 2048);
  bar();
  {
    const unsigned fo = lane < 4 * NT ? WS_FLAG + ((tau0 + (lane >> 2) % NT) * 16 + 4 * kq + (lane & 3)) * 8 : OOB;
    _Pragma("unroll 1") for (unsigned spins = 0; spins < SPIN_CAP; ++spins) {
      const u32x2 f = __builtin_amdgcn_raw_buffer_load_b64(rws, fo, 0, 16);
      if (__all(lane >= 4 * NT || (f[0] == elo && f[1] == ehi))) break;
      __builtin_amdgcn_s_sleep(FLAGSLEEP);
      asm volatile("" ::: "memory");
    }
    asm volatile("" ::: "memory");
  }
  u32x4 of[NT][9];
#pragma unroll
  for (int ks = 0; ks < OPD; ++ks)
#pragma unroll
    for (int tt = 0; tt < NT; ++tt) of[tt][ks] = __builtin_amdgcn_raw_buffer_load_b128(rws, vo, tt * 36864 + ks * 1024, 16);
  SB();
  sfor<0, 36>([&](auto N_) LAM {
    constexpr int n = decltype(N_)::value, nn = n + D;
    if constexpr (nn < 36) wf[nn % W] = trd2(aw[nn & 3] + (nn >> 2) * 4096, aw[nn & 3] + (nn >> 2) * 4096 + 2048);
#pragma unroll
    for (int tt = 0; tt < NT; ++tt) acc[tt][n & 3] = mfma16(wf[n % W], __builtin_bit_cast(s16x8, of[tt][n >> 2]), acc[tt][n & 3]);
    if constexpr ((n & 3) < NT && (n >> 2) + OPD < 9)
      of[n & 3][(n >> 2) + OPD] = __builtin_amdgcn_raw_buffer_load_b128(rws, vo, (n & 3) * 36864 + ((n >> 2) + OPD) * 1024, 16);
    SB();
  });
}

__global__ __launch_bounds__(512, 2) void hedgehog_kernel(const void* x, const void* wA, const void* phiA, const void* woA,
                                                          const void* wB, const void* phiB, const void* woB,
                                                          const void* gate, void* out, void* ws) {
  const unsigned tid = threadIdx.x, lane = tid & 63;
  unsigned long long pc;
  int clen, clen2;
  asm volatile("s_getpc_b64 %0\n.Lhh_start:\n\ts_mov_b32 %1, .Lhh_end1-.Lhh_start\n\ts_mov_b32 %2, .Lhh_end2-.Lhh_start" : "=s"(pc), "=s"(clen), "=s"(clen2));
  clen = clen > clen2 ? clen : clen2;
  u32x2 ev;
  const int w = __builtin_amdgcn_readfirstlane(tid >> 6), j = w & 3;
  const int bid = blockIdx.x, xcd = bid & 7, r = bid >> 3;
  const int tl = r & 7, gl = r >> 3;
  const int t = (xcd & 1) * 8 + tl, grp = (xcd >> 1) * 4 + gl;
  const int br = grp >> 3, gg = grp & 7;
  const unsigned g = lane >> 4, q = (lane & 15) >> 2, p = lane & 3, c16 = lane & 15, c32 = lane & 31, h = lane >> 5;
  const rsrc_t rws = __builtin_amdgcn_make_buffer_rsrc(ws, 0, WS_BYTES, 0x00020000);
  const unsigned ax = (32 * j + c32) * 128 + ((h ^ ((c32 >> 1) & 7)) << 4);
  const int rot = (tl + gl * 2 + (gl >> 1)) % 9;
  const int tau = 8 * t + 2 * j;
  unsigned elo = 0, ehi = 0;

  asm volatile("buffer_load_dwordx2 %0, off, %1, 0 sc1" : "=v"(ev) : "s"(rws) : "memory");
  {
    const rsrc_t rp = __builtin_amdgcn_make_buffer_rsrc((void*)(br ? phiB : phiA), 0, 32 * 648, 0x00020000);
    const rsrc_t rg = __builtin_amdgcn_make_buffer_rsrc((void*)gate, 0, 1152, 0x00020000);
    const rsrc_t rc = __builtin_amdgcn_make_buffer_rsrc((void*)(pc & ~127ull), 0, clen, 0x00020000);
    const unsigned tb = L_TMP + j * 5120;
    asm volatile("s_cmp_ge_u32 %0, 4\n\ts_cbranch_scc1 1f\n\t"
                 "s_mov_b32 m0, %1\n\ts_nop 0\n\tbuffer_load_dwordx4 %2, %3, %4 offen lds\n\t"
                 "s_add_u32 m0, m0, 0x400\n\ts_nop 0\n\tbuffer_load_dwordx4 %2, %3, %5 offen lds\n\t"
                 "s_add_u32 m0, m0, 0x400\n\ts_nop 0\n\tbuffer_load_dwordx4 %2, %3, %6 offen lds\n\t"
                 "s_add_u32 m0, m0, 0x400\n\ts_nop 0\n\tbuffer_load_dwordx4 %2, %7, 0 offen lds\n\t"
                 "s_add_u32 m0, m0, 0x400\n\ts_nop 0\n\tbuffer_load_dwordx4 %2, %7, %8 offen lds\n\t"
                 "s_mov_b32 m0, %9\n\ts_nop 0\n\tbuffer_load_dword %10, %11, %12 offen lds\n1:"
                 :: "s"(w), "s"(tb), "v"(lane * 16), "s"(rp), "s"(gg * 2592), "s"(gg * 2592 + 1024), "s"(gg * 2592 + 2048),
                    "s"(rg), "s"(1024), "s"(L_CNT + 64), "v"(lane < 16 ? lane * 128 : OOB), "s"(rc), "s"(((r & 7) * 4 + j) * 2048)
                 : "memory", "scc");
  }
  int xcd2 = xcd;
  asm volatile("" : "+s"(xcd2));
  const int idx3 = (234 * xcd2 + 7) / 8 + r;
  const bool act3 = idx3 < (234 * (xcd2 + 1) + 7) / 8;
  const int rt = idx3 / 9, ct = idx3 - rt * 9;

  if (__builtin_expect(w >= 4, 1)) {
    const rsrc_t rx = __builtin_amdgcn_make_buffer_rsrc((void*)x, 0, 2048 * 1152, 0x00020000);
    const rsrc_t rw = __builtin_amdgcn_make_buffer_rsrc((void*)(br ? wB : wA), 0, 576 * 3456, 0x00020000);
    auto stage_of = [&](int it) { int s = it + rot; return s >= 9 ? s - 9 : s; };
    unsigned vx[2], vw[5];
#pragma unroll
    for (int e = 0; e < 2; ++e)
      vx[e] = (32 * j + 8 * e + (lane >> 3)) * 1152 + (((lane & 7) ^ ((4 * e + (lane >> 4)) & 7)) << 4);
    auto issue_x = [&](int it) {
      const unsigned sl = ((it + 3) & 3) * SLOT + j * 4096, sx = t * 147456 + stage_of(it) * 128;
      dma16(rx, vx[0], sx, sl);
      dma16(rx, vx[1], sx, sl + 1024);
      dma16(rx, vx[0], sx + 16 * 1152, sl + 2048);
      dma16(rx, vx[1], sx + 16 * 1152, sl + 3072);
    };
    issue_x(0);
    SB();
#pragma unroll
    for (int i = 0; i < 5; ++i) {
      const unsigned c = 64 * i + lane, rp_ = (c * 3641) >> 16, jj = c - rp_ * 18, hi = (jj + 7) >> 4;
      vw[i] = (16 * j + (rp_ & 8) + 4 * (rp_ & 1) + ((rp_ >> 1) & 3)) * 3456 + 1152 + gg * 144 + jj * 16 + hi * (1152 - 144);
    }
    auto issue_w = [&](int it) {
      const unsigned wb = ((it + 3) & 3) * SLOT + XB + j * 4608, sww = stage_of(it) * (64 * 3456);
      dma16(rw, vw[0], sww, wb);
      dma16(rw, vw[1], sww, wb + 1024);
      dma16(rw, vw[2], sww, wb + 2048);
      dma16(rw, vw[3], sww, wb + 3072);
      dma16m(rw, vw[4], sww, wb + 4096, 0xffffffffull);
    };
    issue_w(0);
    issue_x(1); issue_w(1);
    auto wq_voff = [&](unsigned np) {
      const unsigned c = 64 * np + lane, rp_ = (c * 7282) >> 16, jj = c - rp_ * 9;
      return ((rp_ & 48) + 4 * (rp_ & 3) + ((rp_ >> 2) & 3)) * 3456 + gg * 144 + jj * 16;
    };
    unsigned vq0 = wq_voff(2 * j), vq1 = wq_voff(2 * j + 1), vq8 = wq_voff(8);
    KEEP(vq0); KEEP(vq1); KEEP(vq8);
    auto issue_wq = [&](int it) {
      const unsigned so = stage_of(it) * (64 * 3456);
      dma16(rw, vq0, so, it * 9216 + j * 2048);
      dma16(rw, vq1, so, it * 9216 + j * 2048 + 1024);
    };
    auto issue_wq8 = [&](int it) { dma16(rw, vq8, stage_of(it) * (64 * 3456), it * 9216 + 8192); };

    s16x8 xk[36];
    sfor<0, 9>([&](auto IT) LAM {
      constexpr int it = decltype(IT)::value;
      if constexpr (it <= 7) wait_bar<9>(); else wait_bar<4>();
      if constexpr (it == 0) take_epoch(ev, elo, ehi);
      unsigned bx = ax + ((it + 3) & 3) * SLOT;
      KEEP(bx);
#pragma unroll
      for (int ks = 0; ks < 4; ++ks) xk[4 * it + ks] = rd128(bx ^ (ks * 32));
      if constexpr (it + 2 <= 8) { issue_x(it + 2); issue_w(it + 2); }
      else if constexpr (it == 7) { issue_wq(0); issue_wq(1); }
      else {
        issue_wq8(j); issue_wq(2); issue_wq(3);
        lds_signal(C_RING);
        issue_wq8(j + 4);
        issue_wq(4); issue_wq(5); issue_wq(6); issue_wq(7); issue_wq(8);
        issue_wq8(8);
        {
          const rsrc_t ro = __builtin_amdgcn_make_buffer_rsrc((void*)(j >= 2 ? woB : woA), 0, 576 * 1152, 0x00020000);
          const unsigned a = (r * 162 + (j & 1) * 81 + lane) * 128;
          touch(ro, a, 0);
          touch(ro, lane < 17 ? a : OOB, 8192);
        }
      }
      SB();
    });
    const unsigned obase = WS_O + (unsigned)(((tau + (c32 >> 4)) * 36 + (grp >> 2) * 9) * 1024) + c16 * 16;
    unsigned ob = obase + (grp & 3) * 2048 + h * 512, ob2 = obase + 8192 + (grp & 3) * 256 + h * 8;
    unsigned fo = lane < 2 ? WS_FLAG + ((tau + lane) * 16 + grp) * 8 : OOB;
    unsigned ak0, ak2, ak1, bsrc = ((2 * (g & 1) + h) * 16 + c16) * 4;
    {
      const unsigned ho = (c32 >> 2) & 1, rg = (c32 & 3) + 4 * (c32 >> 3);
      const bool ok = ho == h;
      ak0 = ok ? L_KA + (ho * 16 + rg) * 80 : L_KZ;
      ak2 = ok ? L_KA + ((2 + ho) * 16 + rg) * 80 : L_KZ;
      ak1 = (ok && rg < 8 && (rg & 3) < 3) ? L_KB + ((2 * (rg >> 2) + ho) * 3 + (rg & 3)) * 144 : L_KZ;
    }
    KEEP(ob); KEEP(ob2); KEEP(fo); KEEP(ak0); KEEP(ak1); KEEP(ak2); KEEP(bsrc);
    asm volatile("s_waitcnt vmcnt(%0)" :: "i"(14) : "memory");
    lds_signal(C_WQA);
    lds_wait<4>(C_WQA);
    lds_wait<4>(C_KF);
    float gt = __builtin_amdgcn_rcpf(1.f + __builtin_amdgcn_exp2f(*LP(float, L_GATE + (tid & 255) * 4) * (br ? 1.44269504088896340736f : -1.44269504088896340736f)));
    KEEP(gt);
    s16x8 A[11];
    f32x16 qa[2] = {};
    f32x4 ql = {};
    {
      constexpr int D = 4, W = 5, NF = 108;
      unsigned aq = (4 * q + 2 * h) * 144 + (4 * (g & 1) + p) * 8;
      const bool on = (p >> 1) == (g & 1);
      unsigned a20 = on ? (4 * q + 2 * h) * 144 + (16 + (p & 1)) * 8 : L_KZ, a21 = on ? a20 + 144 : L_KZ, st2 = on ? 2304 : 0;
      KEEP(aq); KEEP(a20); KEEP(a21); KEEP(st2);
      s16x8 wf[W];
      sfor<-D, NF>([&](auto N_) LAM {
        constexpr int n = decltype(N_)::value, nn = n + D;
        if constexpr (nn < NF) {
          constexpr int i = nn / 3, m = nn % 3;
          if constexpr (nn == 48) { asm volatile("s_waitcnt vmcnt(%0)" :: "i"(2) : "memory"); lds_signal(C_WQB); lds_wait<4>(C_WQB); }
          if constexpr (nn == 72) { aq += 24 * 2304; KEEP(aq); }
          constexpr unsigned io = (i < 24 ? i : i - 24) * 2304 + m * 64;
          if constexpr (m < 2) wf[nn % W] = trd2(aq + io, aq + io + 144);
          else { wf[nn % W] = trd2(a20, a21); a20 += st2; a21 += st2; KEEP(a20); KEEP(a21); }
          if constexpr (nn >= 90 && nn < 101) A[nn - 90] = rd128(L_AF + (nn - 90) * 1024 + lane * 16);
        }
        if constexpr (n >= 0 && n % 3 < 2) qa[n % 3] = mfma32(wf[n % W], xk[n / 3], qa[n % 3]);
        if constexpr (n >= 0 && n % 3 == 2) ql = mfma16(wf[n % W], xk[n / 3], ql);
        SB();
      });
      KEEP(qa[0]); KEEP(qa[1]); KEEP(ql);
    }
    lds_signal(C_FREE);
    f32x16 pa, pb, pl;
    {
      const unsigned l0 = __builtin_amdgcn_ds_bpermute(bsrc, pk2(ql[0], ql[1])), l1 = __builtin_amdgcn_ds_bpermute(bsrc, pk2(ql[2], ql[3]));
      transform(A, qa[0], qa[1], l0, l1, pa, pb, pl);
    }
    unsigned pq[2][18];
    float ea = 1e-6f * featmap<true, true>(pa, pl[0], pl[1], pq[0], [](auto) {});
#pragma unroll
    for (int k = 0; k < 18; ++k) KEEP(pq[0][k]);
    float eb = 1e-6f * featmap<true, true>(pb, pl[2], pl[3], pq[1], [](auto) {});
#pragma unroll
    for (int k = 0; k < 18; ++k) KEEP(pq[1][k]);
    KEEP(ea); KEEP(eb);
    __builtin_amdgcn_s_setprio(3);
    lds_wait<3>(C_KVT);
    {
      s16x8 k0[5], k2[5], k1[9];
#pragma unroll
      for (int s = 0; s < 5; ++s) { k1[s] = rd128(ak1 + 16 * s); k0[s] = rd128(ak0 + 16 * s); }
#pragma unroll
      for (int s = 4; s < 9; ++s) { if (s > 4) k1[s] = rd128(ak1 + 16 * s); k2[s - 4] = rd128(ak2 + 16 * (s - 4)); }
      SB();
      const unsigned* pf = &pq[0][0];
      f32x16 n0 = {}, n1 = {}, n2 = {}, n3 = {};
#pragma unroll
      for (int s = 0; s < 5; ++s) {
        const s16x8 b = mk8(pf[4 * s], pf[4 * s + 1], pf[4 * s + 2], pf[4 * s + 3]);
        n1 = mfma32(k1[s], b, n1);
        n0 = mfma32(k0[s], b, n0);
      }
      SB();
      float o[36];
      float sa = 0.f;
      sfor<4, 9>([&](auto S_) LAM {
        constexpr int s = decltype(S_)::value;
        const s16x8 b = mk8(pf[4 * s], pf[4 * s + 1], pf[4 * s + 2], pf[4 * s + 3]);
        n3 = mfma32(k1[s], b, n3);
        n2 = mfma32(k2[s - 4], b, n2);
        if constexpr (s == 5) { sa = gt * __builtin_amdgcn_rcpf(n1[2] + ea); OPQ(sa); }
        if constexpr (s == 6 || s == 7) {
          constexpr int u = s - 6;
#pragma unroll
          for (int k = 0; k < 8; ++k) { o[8 * u + k] = n0[8 * u + k] * sa; OPQ(o[8 * u + k]); }
          __builtin_amdgcn_raw_buffer_store_b128(u32x4{pk2(o[8 * u], o[8 * u + 1]), pk2(o[8 * u + 2], o[8 * u + 3]), pk2(o[8 * u + 4], o[8 * u + 5]), pk2(o[8 * u + 6], o[8 * u + 7])},
                                                 rws, ob + u * 256, 0, 16);
        }
        SB();
      });
      const float sb = gt * __builtin_amdgcn_rcpf(n3[6] + eb);
#pragma unroll
      for (int k = 0; k < 16; ++k) o[18 + k] = n2[k] * sb;
      o[16] = n1[0] * sa; o[17] = n1[1] * sa; o[34] = n3[4] * sb; o[35] = n3[5] * sb;
#pragma unroll
      for (int u = 2; u < 4; ++u)
        __builtin_amdgcn_raw_buffer_store_b128(u32x4{pk2(o[8 * u], o[8 * u + 1]), pk2(o[8 * u + 2], o[8 * u + 3]), pk2(o[8 * u + 4], o[8 * u + 5]), pk2(o[8 * u + 6], o[8 * u + 7])},
                                               rws, ob + 1024 + (u & 1) * 256, 0, 16);
      __builtin_amdgcn_raw_buffer_store_b64(u32x2{pk2(o[32], o[33]), pk2(o[34], o[35])}, rws, ob2, 0, 16);
      drain();
      __builtin_amdgcn_raw_buffer_store_b64(u32x2{elo, ehi}, rws, fo, 0, 16);
    }
  } else {
    __builtin_amdgcn_s_setprio(2);
    {
      const unsigned tb = L_TMP + j * 5120;
      if (tid < (L_CNT + 64 - L_KA) / 32) { *LP(u32x4, L_KA + tid * 32) = u32x4{0u, 0u, 0u, 0u}; *LP(u32x4, L_KA + tid * 32 + 16) = u32x4{0u, 0u, 0u, 0u}; }
      *LP(unsigned, L_ONES + tid * 4) = 0x3f803f80u;
      const unsigned ln = tid >> 2, jp = tid & 3, i = ln & 31, hk = ln >> 5;
      const unsigned ho = (i >> 2) & 1, rg_ = (i & 3) + 4 * (i >> 3);
      const unsigned cb = 8 * (jp >> 1) + 4 * hk + 2 * (jp & 1);
      unsigned am = tb + (cb * 18 + rg_) * 2, al = tb + (cb * 18 + 16 + (rg_ & 1)) * 2;
      const unsigned dm = cb - 18 * ho, dl = rg_ < 4 ? cb - 18 * ((rg_ & 2) + ho) : 0x1000u;
      unsigned ag = tid + 64 * rot;
      KEEP(am); KEEP(al);
      asm volatile("s_waitcnt vmcnt(1)" ::: "memory");
#pragma unroll
      for (int f = 0; f < 11; ++f) {
        const int s = f < 3 ? f : f < 6 ? f - 1 : f - 6;
        const unsigned a = (f < 6 ? am : al) + s * 576;
        const unsigned v = (unsigned)*LP(unsigned short, a) | ((unsigned)*LP(unsigned short, a + 36) << 16);
        const unsigned d = f < 3 ? dm + 16 * s : f < 6 ? dm + 16 * s - 36 : dl + 16 * s;
        *LP(unsigned, L_AF + f * 1024 + tid * 4) = d < 18u ? v : 0u;
      }
#pragma unroll
      for (int k = 0; k < 3; ++k) {
        const unsigned src = ag + 256 * k, v = *LP(unsigned short, tb + 3072 + 2 * (src >= 576 ? src - 576 : src)) * 0x10001u;
        *LP(u32x2, k < 2 || tid < 64 ? L_GATE + (tid + 256 * k) * 8 : L_SINK) = u32x2{v, v};
      }
    }
    const unsigned aw0 = XB + h * 2304 + q * 576 + (4 * (g & 1) + p) * 8;
    unsigned aph[4], av[2], rp_, rv0, rvL, h1a, h1b, h1c, h1d;
    {
      const unsigned s0 = 32 * j + c32;
#pragma unroll
      for (int k = 0; k < 4; ++k) { aph[k] = L_PHI + 9 * h * 1024 + ((s0 ^ (8 * ((k + h) & 3))) << 3); KEEP(aph[k]); }
#pragma unroll
      for (int k = 0; k < 2; ++k) { av[k] = L_V + h * 1024 + ((s0 ^ (8 * (2 * k + h))) << 3); KEEP(av[k]); }
      const unsigned tk = 4 * g + q, vp = 9 * (j >> 1) + ((j & 1) ? 5 : 0) + p, vl = 9 * (j >> 1) + 4;
      rp_ = L_PHI + (9 * j + p) * 1024 + ((tk ^ (8 * ((j + p) & 3))) << 3);
      rv0 = L_V + vp * 1024 + ((tk ^ (8 * (vp & 3))) << 3);
      rvL = (p == 0 ? L_V + vl * 1024 : L_ONES) + ((tk ^ (8 * ((vl + p) & 3))) << 3);
      const unsigned hb = WS_H1 + ((grp * 16 + 4 * j) * 16 + t) * 768 + c16 * 16;
      h1a = hb + g * 12288;
      h1b = g == 0 ? hb + 512 : OOB;
      h1c = g < 2 ? hb + (1 + g) * 12288 + 512 : OOB;
      h1d = (g < 2 && c16 < 4) ? hb + 3 * 12288 + 512 + g * 64 : OOB;
      KEEP(rp_); KEEP(rv0); KEEP(rvL); KEEP(h1a); KEEP(h1b); KEEP(h1c); KEEP(h1d);
    }
    asm volatile("s_waitcnt lgkmcnt(0)" ::: "memory");
    take_epoch(ev, elo, ehi);
    unsigned tagv = elo; KEEP(tagv);

    f32x16 acc[5] = {};
    s16x8 A[11];
    {
      constexpr int D = 5, W = 6, NF = 180;
      s16x8 wf[W], xf[2];
      unsigned bx = 0, b0 = 0;
      const bool zl = g & 1, gl_ = zl && p < 2;
      unsigned a40 = gl_ ? L_GATE + (8 * h + q) * 8 : zl ? L_KZ : 3 * SLOT + aw0 + 256, a41 = gl_ ? a40 + 32 : zl ? L_KZ : a40 + 288;
      const unsigned sz = gl_ ? 128 : 0;
      unsigned s42 = zl ? sz : 4608, s43 = zl ? sz : 4608 + XB, s44 = zl ? sz : 4608 + XB - 4 * SLOT;
      KEEP(a40); KEEP(a41); KEEP(s42); KEEP(s43); KEEP(s44);
      sfor<-D, NF>([&](auto N_) LAM {
        constexpr int n = decltype(N_)::value, nn = n + D;
        if constexpr (nn < NF) {
          constexpr int kst = nn / 5, m = nn % 5, it = kst >> 2, ks = kst & 3;
          if constexpr (m == 0 && ks == 0) {
            asm volatile("s_waitcnt lgkmcnt(8)\n\ts_barrier" ::: "memory");
            bx = ax + ((it + 3) & 3) * SLOT; b0 = aw0 + ((it + 3) & 3) * SLOT;
            KEEP(bx); KEEP(b0);
          }
          if constexpr (m == 0) xf[kst & 1] = rd128(bx ^ (ks * 32));
          if constexpr (m < 4) wf[nn % W] = trd2(b0 + ks * 4608 + m * 64, b0 + ks * 4608 + m * 64 + 288);
          else {
            wf[nn % W] = trd2(a40, a41);
            const unsigned st = ks < 3 ? s42 : ((it + 3) & 3) == 3 ? s44 : s43;
            a40 += st; a41 += st; KEEP(a40); KEEP(a41);
          }
          if constexpr (nn >= 141 && nn < 152) A[nn - 141] = rd128(L_AF + (nn - 141) * 1024 + lane * 16);
        }
        if constexpr (n >= 0) acc[n % 5] = mfma32(wf[n % W], xf[(n / 5) & 1], acc[n % 5]);
        SB();
      });
    }
    lds_signal(C_RING);
    {
      f32x16 pa, pb, pl;
      transform(A, acc[0], acc[1], pk2(acc[2][0], acc[2][1]), pk2(acc[2][2], acc[2][3]), pa, pb, pl);
      u32x2 vv[9];
#pragma unroll
      for (int n = 0; n < 9; ++n) {
        const int rgp = n + 1;
        const f32x16& tl_ = acc[2 + (rgp >> 2)];
        vv[n] = u32x2{pk2(tl_[4 * (rgp & 3)], tl_[4 * (rgp & 3) + 1]), pk2(tl_[4 * (rgp & 3) + 2], tl_[4 * (rgp & 3) + 3])};
        KEEP(vv[n]);
      }
      KEEP(pa); KEEP(pl); KEEP(pb);
      lds_wait<8>(C_RING);
      *LP(float, L_GATE + tid * 4) = acc[4][8];
      unsigned ph[18], ph2[18];
      featmap<true>(pa, pl[0], pl[1], ph, [&](auto E_) LAM {
        constexpr int e = decltype(E_)::value;
        SB(); *LP(u32x2, av[e & 1] + 2 * e * 1024) = vv[e]; SB();
      });
#pragma unroll
      for (int k = 0; k < 18; ++k) KEEP(ph[k]);
      lds_signal(C_KF);
      featmap<false>(pb, pl[2], pl[3], ph2, [&](auto E_) LAM {
        constexpr int e = decltype(E_)::value;
        SB(); *LP(u32x2, aph[e & 3] + e * 1024) = u32x2{ph[2 * e], ph[2 * e + 1]}; SB();
      });
#pragma unroll
      for (int e = 0; e < 9; ++e) *LP(u32x2, aph[(e + 2) & 3] + (18 + e) * 1024) = u32x2{ph2[2 * e], ph2[2 * e + 1]};
    }
    lds_signal(C_PHI);
    lds_wait<4>(C_PHI);
    {
      s16x8 v0[4], vl[4], pa[4][3];
#pragma unroll
      for (int kk = 0; kk < 4; ++kk) {
        v0[kk] = trd2(rv0 + kk * 256, (rv0 ^ 128) + kk * 256);
        vl[kk] = trd2(rvL + kk * 256, (rvL ^ 128) + kk * 256);
#pragma unroll
        for (int m = 0; m < 3; ++m) pa[kk][m] = trd2(rp_ + m * 4096 + kk * 256, (rp_ ^ 128) + m * 4096 + kk * 256);
      }
      SB();
      f32x4 d0[3] = {}, d1[3] = {};
#pragma unroll
      for (int kk = 0; kk < 4; ++kk)
#pragma unroll
        for (int m = 0; m < 3; ++m) {
          d0[m] = mfma16(pa[kk][m], v0[kk], d0[m]);
          d1[m] = mfma16(vl[kk], pa[kk][m], d1[m]);
        }
      __builtin_amdgcn_raw_buffer_store_b128(unit(pk2(d0[0][0], d0[0][1]), pk2(d0[0][2], d0[0][3]), tagv), rws, h1a, 0, 16);
      __builtin_amdgcn_raw_buffer_store_b128(unit(pk2(d0[1][0], d0[1][1]), pk2(d0[1][2], d0[1][3]), tagv), rws, h1a + 256, 0, 16);
      __builtin_amdgcn_raw_buffer_store_b128(unit(pk2(d0[2][0], d0[2][1]), pk2(d0[2][2], d0[2][3]), tagv), rws, h1b, 0, 16);
      unsigned l0, l1, l2;
      if (j & 1) { l0 = pk2(d1[0][2], d1[0][3]); l1 = pk2(d1[1][2], d1[1][3]); l2 = pk2(d1[2][2], d1[2][3]); }
      else       { l0 = pk2(d1[0][0], d1[0][1]); l1 = pk2(d1[1][0], d1[1][1]); l2 = pk2(d1[2][0], d1[2][1]); }
      __builtin_amdgcn_raw_buffer_store_b128(unit(l0, l1, tagv), rws, h1c, 0, 16);
      __builtin_amdgcn_raw_buffer_store_b128(unit(l2, l2, tagv), rws, h1d, 0, 16);
    }
    lds_signal(C_KD);
    const rsrc_t roA = __builtin_amdgcn_make_buffer_rsrc((void*)woA, 0, act3 ? 576 * 1152 : 0, 0x00020000);
    const rsrc_t roB = __builtin_amdgcn_make_buffer_rsrc((void*)woB, 0, act3 ? 576 * 1152 : 0, 0x00020000);
    auto wo_voff = [&](unsigned e, unsigned (&vo)[3]) LAM {
      const unsigned gq = 2 * (e & 1) + (lane >> 5), jj = 4 * (e >> 1) + ((lane >> 3) & 3);
      const unsigned ch = ((lane & 7) ^ (2 * ((lane >> 4) & 3))) * 16 + ct * 128;
      const unsigned v1 = 16 + 8 * (gq & 1) + jj;
      vo[0] = (18 * (e & 1) + 8 * (gq & 1) + jj) * 1152 + ch;
      vo[1] = (18 * (e & 1) + v1 + (v1 >= 18 ? 18 : 0)) * 1152 + ch;
      vo[2] = (72 * gq + 18 * (e >> 1) + 50 + (jj & 3)) * 1152 + ch;
    };
    auto issue_wo = [&](int b_, const unsigned (&vo)[3], unsigned e) LAM {
      const int kq = b_ / 9, blk = b_ % 9, k = blk < 8 ? (blk & 1) : 2;
      dma16(kq >= 2 ? roB : roA, vo[k], ((kq & 1) * 288 + (blk < 8 ? 72 * (blk >> 1) : 0)) * 1152, b_ * 4096 + e * 1024);
    };
    if (j == 3) {
      unsigned vo[4][3];
#pragma unroll
      for (int e = 0; e < 4; ++e) wo_voff(e, vo[e]);
      lds_wait<4>(C_KD);
      sfor<21, 34>([&](auto B_) LAM { _Pragma("unroll") for (int e = 0; e < 4; ++e) issue_wo(decltype(B_)::value, vo[e], e); });
    } else {
      const bool val1 = j < 2 || (t & 3) != 3 || c16 < 8;
      unsigned r1 = val1 ? WS_H1 + ((grp * 16 + t) * 16 + 4 * g) * 768 + (16 * j + c16) * 16 : OOB;
      unsigned w2 = (val1 && g == 0) ? WS_H2 + (((grp * 3 + j) * 4 + (t >> 2)) * 64 + (t & 3) * 16 + c16) * 16 : OOB;
      KEEP(r1); KEEP(w2);
      __builtin_amdgcn_s_sleep(D1);
      {
        u32x4 gv[4];
#pragma unroll
        for (int k = 0; k < 4; ++k) gv[k] = u32x4{0u, val1 ? ~tagv : tagv, 0u, tagv};
        _Pragma("unroll 1") for (unsigned spins = 0; spins < SPIN_CAP; ++spins) {
#pragma unroll
          for (int k = 0; k < 4; ++k)
            if (gv[k][1] != tagv || gv[k][3] != tagv) gv[k] = __builtin_amdgcn_raw_buffer_load_b128(rws, r1 + k * 768, 0, 16);
          unsigned bad = 0;
#pragma unroll
          for (int k = 0; k < 4; ++k) bad |= (gv[k][1] ^ tagv) | (gv[k][3] ^ tagv);
          if (__all(bad == 0)) break;
          __builtin_amdgcn_s_sleep(HOPSLEEP);
          asm volatile("" ::: "memory");
        }
        float sm[4];
        sm[0] = addf(addf(blo(gv[0][0]), blo(gv[1][0])), addf(blo(gv[2][0]), blo(gv[3][0])));
        sm[1] = addf(addf(bhi(gv[0][0]), bhi(gv[1][0])), addf(bhi(gv[2][0]), bhi(gv[3][0])));
        sm[2] = addf(addf(blo(gv[0][2]), blo(gv[1][2])), addf(blo(gv[2][2]), blo(gv[3][2])));
        sm[3] = addf(addf(bhi(gv[0][2]), bhi(gv[1][2])), addf(bhi(gv[2][2]), bhi(gv[3][2])));
        asm volatile("s_nop 1" : "+v"(sm[0]), "+v"(sm[1]), "+v"(sm[2]), "+v"(sm[3]));
#pragma unroll
        for (int k = 0; k < 4; ++k) { F2 a = sw16(sm[k]); sm[k] = addf(a.a, a.b); }
        asm volatile("s_nop 1" : "+v"(sm[0]), "+v"(sm[1]), "+v"(sm[2]), "+v"(sm[3]));
#pragma unroll
        for (int k = 0; k < 4; ++k) { F2 a = sw32(sm[k]); sm[k] = addf(a.a, a.b); }
        __builtin_amdgcn_raw_buffer_store_b128(unit(pk2(sm[0], sm[1]), pk2(sm[2], sm[3]), tagv), rws, w2, 0, 16);
      }
      {
        unsigned r2 = WS_H2 + ((grp * 3 + j) * 256 + lane) * 16;
        unsigned le[4], lo_[4], se, so;
        if (j < 2) {
          le[0] = L_KA + c16 * 80 + g * 8 + 32 * j; se = 2560 + 8;
          lo_[0] = (c16 < 14 ? L_KA + (18 + c16) * 80 : L_KB + (c16 - 11) * 144) + g * 8 + 32 * j; so = c16 < 14 ? 2560 + 8 : 864 + 72;
#pragma unroll
          for (int k = 1; k < 4; ++k) { le[k] = le[0] + 2 * k; lo_[k] = lo_[0] + 2 * k; }
        } else {
          const bool isd = g == 1 || (g == 3 && c16 < 4), dead = g == 3 && c16 >= 8;
          const unsigned fo = g == 3 ? 64 + 2 * (c16 & 3) : 2 * c16, f2 = g == 3 ? 0 : 32;
          if (g == 0) {
            le[0] = L_KA + c16 * 80 + 64; se = 2560 + 8;
            lo_[0] = (c16 < 14 ? L_KA + (18 + c16) * 80 : L_KB + (c16 - 11) * 144) + 64; so = c16 < 14 ? 2560 + 8 : 864 + 72;
#pragma unroll
            for (int k = 1; k < 4; ++k) { le[k] = le[0] + 2 * k; lo_[k] = lo_[0] + 2 * k; }
          } else {
            le[0] = L_KB + (isd ? 0 : 288) + fo; le[1] = le[0] + (isd ? 144 : 0); le[2] = le[0] + f2; le[3] = le[1] + f2; se = 864 + 72;
            lo_[0] = (isd ? L_KA + 16 * 80 : L_KB + 5 * 144) + fo; lo_[1] = lo_[0] + (isd ? 80 : 0); lo_[2] = lo_[0] + f2; lo_[3] = lo_[1] + f2;
            so = isd ? 2560 + 8 : 864 + 72;
            if (dead) { se = 0; so = 0; _Pragma("unroll") for (int k = 0; k < 4; ++k) { le[k] = L_SINK; lo_[k] = L_SINK; } }
          }
        }
        unsigned le2[4], lo2[4];
#pragma unroll
        for (int k = 0; k < 4; ++k) { le2[k] = le[k] + se; lo2[k] = lo_[k] + so; KEEP(le[k]); KEEP(lo_[k]); KEEP(le2[k]); KEEP(lo2[k]); }
        KEEP(r2);
        __builtin_amdgcn_s_sleep(D2);
        u32x4 gv[4];
#pragma unroll
        for (int k = 0; k < 4; ++k) gv[k] = u32x4{0u, (j < 2 || lane < 56) ? ~tagv : tagv, 0u, tagv};
        _Pragma("unroll 1") for (unsigned spins = 0; spins < SPIN_CAP; ++spins) {
#pragma unroll
          for (int k = 0; k < 4; ++k)
            if (gv[k][1] != tagv || gv[k][3] != tagv) gv[k] = __builtin_amdgcn_raw_buffer_load_b128(rws, r2 + k * 1024, 0, 16);
          unsigned bad = 0;
#pragma unroll
          for (int k = 0; k < 4; ++k) bad |= (gv[k][1] ^ tagv) | (gv[k][3] ^ tagv);
          if (__all(bad == 0)) break;
          __builtin_amdgcn_s_sleep(HOPSLEEP);
          asm volatile("" ::: "memory");
        }
        if (j < 2) {
          *LP(unsigned, le[0]) = gv[0][0]; *LP(unsigned, le[0] + 4) = gv[0][2];
          *LP(unsigned, lo_[0]) = gv[1][0]; *LP(unsigned, lo_[0] + 4) = gv[1][2];
          *LP(unsigned, le2[0]) = gv[2][0]; *LP(unsigned, le2[0] + 4) = gv[2][2];
          *LP(unsigned, lo2[0]) = gv[3][0]; *LP(unsigned, lo2[0] + 4) = gv[3][2];
        } else {
#pragma unroll
          for (int hd = 0; hd < 4; ++hd) {
            const unsigned* la = hd == 0 ? le : hd == 1 ? lo_ : hd == 2 ? le2 : lo2;
            *LP(unsigned short, la[0]) = (unsigned short)gv[hd][0];
            *LP(unsigned short, la[1]) = (unsigned short)(gv[hd][0] >> 16);
            *LP(unsigned short, la[2]) = (unsigned short)gv[hd][2];
            *LP(unsigned short, la[3]) = (unsigned short)(gv[hd][2] >> 16);
          }
        }
      }
      lds_signal(C_KVT);
    }
    __builtin_amdgcn_s_setprio(0);
    {
      unsigned vo[3];
      wo_voff(j, vo);
      lds_wait<4>(C_FREE);
      lds_wait<3>(C_KVT);
      sfor<0, 21>([&](auto B_) LAM { issue_wo(decltype(B_)::value, vo, j); });
      issue_wo(34, vo, j); issue_wo(35, vo, j);
      asm volatile("s_waitcnt vmcnt(0)\n.Lhh_end1:" ::: "memory");
    }
  }
  if (!act3) return;
  __builtin_amdgcn_s_setprio(0);
  bar();

  {
    const int kq = j, th = w >> 2;
    const int tau0 = 5 * rt + 3 * th;
    const int nt = th == 0 ? 3 : (rt < 25 ? 2 : 0);
    f32x4 a3[3][4] = {};
    if (nt == 3) p3_body<3>(rws, lane, kq, tau0, elo, ehi, a3);
    else if (nt == 2) p3_body<2>(rws, lane, kq, tau0, elo, ehi, a3);
    else bar();
    {
      const unsigned rb = kq * 36864 + (th ? 0 : 8192) + c16 * 256;
#pragma unroll
      for (int n = 0; n < 4; ++n) {
        const unsigned wa = rb + (((4 * (n ^ kq) + g) ^ c16) << 4);
#pragma unroll
        for (int a = 0; a < 3; ++a)
          if (a < nt) *LP(f32x4, wa + a * 4096) = a3[a][n];
      }
    }
    bar();
    const rsrc_t rout = __builtin_amdgcn_make_buffer_rsrc(out, 0, 2048 * 1152, 0x00020000);
    auto finish = [&](unsigned row) LAM {
      const unsigned ra = (row < 48 ? 8192 + row * 256 : (row - 48) * 256) + (((2 * (tid & 7)) ^ (row & 15)) << 4);
      f32x4 s0 = *LP(f32x4, ra), s1 = *LP(f32x4, ra ^ 16);
#pragma unroll
      for (int k = 1; k < 4; ++k) { s0 += *LP(f32x4, ra + k * 36864); s1 += *LP(f32x4, (ra ^ 16) + k * 36864); }
      __builtin_amdgcn_raw_buffer_store_b128(u32x4{pk2(s0[0], s0[1]), pk2(s0[2], s0[3]), pk2(s1[0], s1[1]), pk2(s1[2], s1[3])}, rout,
                                             (80 * rt + row) * 1152 + ct * 128 + (tid & 7) * 16, 0, 0);
    };
    finish(tid >> 3);
    if (w < 2) finish(64 + (tid >> 3));
    asm volatile(".Lhh_end2:" ::: "memory");
    if (bid == 0 && tid == 0) __builtin_amdgcn_raw_buffer_store_b64(u32x2{elo, ehi}, rws, WS_EP, 0, 16);
  }
}

extern "C" void kernel_launch(void* const* d_in, const int* in_sizes, int n_in, void* d_out, int out_size, void* d_ws,
                              size_t ws_size, hipStream_t stream) {
  (void)hipFuncSetAttribute((const void*)hedgehog_kernel, hipFuncAttributeMaxDynamicSharedMemorySize, LDS_BYTES);
  hedgehog_kernel<<<256, 512, LDS_BYTES, stream>>>(d_in[0], d_in[1], d_in[2], d_in[3], d_in[5], d_in[6], d_in[7], d_in[8],
                                                   d_out, d_ws);
}
```
